```python
import torch

def _softmax(x, axis=-1):
    return torch.softmax(x, dim=axis)
def _gelu(x):
    return torch.nn.functional.gelu(x, approximate="tanh")
def _silu(x):
    return torch.nn.functional.silu(x)
def _sigmoid(x):
    return torch.sigmoid(x)
def _layernorm(x, axis=-1, eps=1e-5):
    m = x.mean(dim=axis, keepdim=True)
    v = x.var(dim=axis, keepdim=True, unbiased=False)
    return (x - m) / torch.sqrt(v + eps)
def _scatter_add(tgt, idx, src, axis=0):
    return tgt.clone().index_add_(axis, idx, src)
def _segment_sum(src, seg, n):
    out = torch.zeros((n,) + src.shape[1:], dtype=src.dtype)
    return out.index_add_(0, seg, src)
def _pad(x, pw):
    flat = tuple(v for pair in reversed(pw) for v in pair)
    return torch.nn.functional.pad(x, flat)

kernel_name = 'concat_heads__grouped_kv_mqa__spectral_fnet__prompt'

def setup_inputs():

    _w = (lambda a: (lambda t: t.to(torch.bfloat16) if t.is_floating_point() else t)(torch.as_tensor(a)))
    def _A_setup():
        B, S, D, H = 1, 2048, 576, 32
        Dh = D // H
        return {
            "x": _w((lambda *s: torch.randn(*s, dtype=torch.bfloat16) * 0.05)(B, S, D)),
            "wq": _w((lambda *s: torch.randn(*s, dtype=torch.bfloat16) * 3.2)(D, H * Dh)),
            "wk": _w((lambda *s: torch.randn(*s, dtype=torch.bfloat16) * 3.2)(D, Dh)),
            "wv": _w((lambda *s: torch.randn(*s, dtype=torch.bfloat16) * 0.4)(D, Dh)),
            "wo": _w((lambda *s: torch.randn(*s, dtype=torch.bfloat16) * 0.4)(H * Dh, D)),
        }
    _A_in = _A_setup()

    def _B_setup():
        B, S, D = 1, 2048, 576
        ns = torch.arange(S)[:, None] * torch.arange(S)[None, :]
        nd = torch.arange(D)[:, None] * torch.arange(D)[None, :]
        Ws_re = torch.cos(-2.0 * torch.pi * ns / S).to(float)
        Ws_im = torch.sin(-2.0 * torch.pi * ns / S).to(float)
        Wd_re = torch.cos(-2.0 * torch.pi * nd / D).to(float)
        Wd_im = torch.sin(-2.0 * torch.pi * nd / D).to(float)
        return {
            "x": _w((lambda *s: torch.randn(*s, dtype=torch.bfloat16) * 0.05)(B, S, D)),
            "dft_seq_re": _w(Ws_re),
            "dft_seq_im": _w(Ws_im),
            "dft_hid_re": _w(Wd_re),
            "dft_hid_im": _w(Wd_im),
        }
    _B_in = _B_setup()

    return {
        "A_x": _A_in["x"],
        "A_wq": _A_in["wq"],
        "A_wk": _A_in["wk"],
        "A_wv": _A_in["wv"],
        "A_wo": _A_in["wo"],
        "B_x": _B_in["x"],
        "B_dft_seq_re": _B_in["dft_seq_re"],
        "B_dft_seq_im": _B_in["dft_seq_im"],
        "B_dft_hid_re": _B_in["dft_hid_re"],
        "B_dft_hid_im": _B_in["dft_hid_im"],
    }

def reference(A_x, A_wq, A_wk, A_wv, A_wo, B_x, B_dft_seq_re, B_dft_seq_im, B_dft_hid_re, B_dft_hid_im):
    def _A_ref(x, wq, wk, wv, wo):
        B, S, D = x.shape
        H = 32
        Dh = D // H
        q = (lambda x, *d: x.permute(*d))((x @ wq).reshape(B, S, H, Dh), 0, 2, 1, 3)
        k = x @ wk
        v = x @ wv
        scores = torch.einsum("bhqd,bkd->bhqk", q, k) * (Dh ** -0.5)
        p = _softmax(scores, axis=-1)
        o = torch.einsum("bhqk,bkd->bhqd", p, v)
        return (lambda x, *d: x.permute(*d))(o, 0, 2, 1, 3).reshape(B, S, H * Dh) @ wo

    def _B_ref(x, dft_seq_re, dft_seq_im, dft_hid_re, dft_hid_im):
        a = torch.einsum("bsd,ed->bse", x, dft_hid_re)
        b = torch.einsum("bsd,ed->bse", x, dft_hid_im)
        y = torch.einsum("ts,bse->bte", dft_seq_re, a) - torch.einsum("ts,bse->bte", dft_seq_im, b)
        return y

    a = _A_ref(A_x, A_wq, A_wk, A_wv, A_wo)
    b = _B_ref(B_x, B_dft_seq_re, B_dft_seq_im, B_dft_hid_re, B_dft_hid_im)
    return torch.concatenate([a, b], axis=-1)
```

```cpp
#include <hip/hip_runtime.h>
#include <hip/hip_bf16.h>
typedef __hip_bfloat16 bf16;
#define S 2048
#define D 576
#define H 32
#define DH 18
__device__ inline float b2f(bf16 x){return __bfloat162float(x);}
__global__ void gemm_naive(const bf16* A,int lda,const bf16* B,int ldbk,int ldbn,bf16* C,int ldc,int M,int N,int K){
  int n=blockIdx.x*blockDim.x+threadIdx.x; int m=blockIdx.y;
  if(n>=N) return;
  float acc=0.f;
  for(int k=0;k<K;k++) acc+=b2f(A[(long)m*lda+k])*b2f(B[(long)k*ldbk+(long)n*ldbn]);
  C[(long)m*ldc+n]=__float2bfloat16(acc);
}
__global__ void gemm_b2(const bf16* Cr,const bf16* Ci,const bf16* a,const bf16* b,bf16* out){
  int n=blockIdx.x*blockDim.x+threadIdx.x; int m=blockIdx.y;
  if(n>=D) return;
  float acc=0.f;
  for(int k=0;k<S;k++) acc+=b2f(Cr[(long)m*S+k])*b2f(a[(long)k*D+n])-b2f(Ci[(long)m*S+k])*b2f(b[(long)k*D+n]);
  out[(long)m*2*D+D+n]=__float2bfloat16(acc);
}
__global__ void attn_naive(const bf16* q,const bf16* k,const bf16* v,bf16* o){
  int idx=blockIdx.x*blockDim.x+threadIdx.x;
  int s=idx/H,h=idx%H;
  float qq[DH]; for(int d=0;d<DH;d++) qq[d]=b2f(q[(long)s*D+h*DH+d]);
  float m=-1e30f,l=0.f; float acc[DH]; for(int d=0;d<DH;d++) acc[d]=0.f;
  const float sc=0.23570226039551584f;
  for(int j=0;j<S;j++){
    float sv=0.f;
    #pragma unroll
    for(int d=0;d<DH;d++) sv+=qq[d]*b2f(k[j*DH+d]);
    sv*=sc;
    float mn=fmaxf(m,sv); float al=__expf(m-mn); float p=__expf(sv-mn);
    l=l*al+p;
    #pragma unroll
    for(int d=0;d<DH;d++) acc[d]=acc[d]*al+p*b2f(v[j*DH+d]);
    m=mn;
  }
  for(int d=0;d<DH;d++) o[(long)s*D+h*DH+d]=__float2bfloat16(acc[d]/l);
}
extern "C" void kernel_launch(void* const* d_in, const int* in_sizes, int n_in, void* d_out, int out_size, void* d_ws, size_t ws_size, hipStream_t stream){
  const bf16* Ax=(const bf16*)d_in[0]; const bf16* wq=(const bf16*)d_in[1]; const bf16* wk=(const bf16*)d_in[2];
  const bf16* wv=(const bf16*)d_in[3]; const bf16* wo=(const bf16*)d_in[4]; const bf16* Bx=(const bf16*)d_in[5];
  const bf16* Cr=(const bf16*)d_in[6]; const bf16* Ci=(const bf16*)d_in[7]; const bf16* Dr=(const bf16*)d_in[8]; const bf16* Di=(const bf16*)d_in[9];
  bf16* out=(bf16*)d_out; bf16* ws=(bf16*)d_ws;
  bf16* q=ws; bf16* k=q+S*D; bf16* v=k+S*32; bf16* o=v+S*32; bf16* a=o+S*D; bf16* b=a+S*D;
  gemm_naive<<<dim3(9,S),64,0,stream>>>(Ax,D,wq,D,1,q,D,S,D,D);
  gemm_naive<<<dim3(1,S),64,0,stream>>>(Ax,D,wk,DH,1,k,DH,S,DH,D);
  gemm_naive<<<dim3(1,S),64,0,stream>>>(Ax,D,wv,DH,1,v,DH,S,DH,D);
  attn_naive<<<S*H/64,64,0,stream>>>(q,k,v,o);
  gemm_naive<<<dim3(9,S),64,0,stream>>>(o,D,wo,D,1,out,2*D,S,D,D);
  gemm_naive<<<dim3(9,S),64,0,stream>>>(Bx,D,Dr,1,D,a,D,S,D,D);
  gemm_naive<<<dim3(9,S),64,0,stream>>>(Bx,D,Di,1,D,b,D,S,D,D);
  gemm_b2<<<dim3(9,S),64,0,stream>>>(Cr,Ci,a,b,out);
}
```

```cpp
#include <hip/hip_runtime.h>
#include <hip/hip_bf16.h>

typedef __attribute__((ext_vector_type(16))) float f32x16;
typedef __attribute__((ext_vector_type(8))) float f32x8;
typedef __attribute__((ext_vector_type(4))) float f32x4;
typedef __attribute__((ext_vector_type(2))) float f32x2;
typedef __attribute__((ext_vector_type(8))) __bf16 bf16x8;
typedef __attribute__((ext_vector_type(16))) __bf16 bf16x16;
typedef __attribute__((ext_vector_type(2))) __bf16 bf16x2;
typedef __attribute__((ext_vector_type(4))) short s16x4;
typedef __attribute__((ext_vector_type(8))) unsigned short u16x8;
typedef __attribute__((ext_vector_type(4))) unsigned u32x4;
typedef __attribute__((ext_vector_type(2))) unsigned u32x2;
typedef __amdgpu_buffer_rsrc_t rsrc_t;
typedef unsigned long long u64;
#define LAS __attribute__((address_space(3)))
#define GAS __attribute__((address_space(1)))
#if defined(__HIP_DEVICE_COMPILE__)
#define NOPK __attribute__((target("no-packed-fp32-ops")))
#else
#define NOPK
#endif
#define DI __device__ __forceinline__
#define SBAR() __builtin_amdgcn_sched_barrier(0)

constexpr int SEQ = 2048, DM = 576, ROWB = DM * 2;
constexpr int KVT = 2560;
constexpr float MSHIFT = 64.f;
constexpr float QSCALE = 0.23570226039551584f * 1.4426950408889634f;
constexpr int LDS_BYTES = 81920;
constexpr unsigned CODE_BYTES = 26624;
#define PRIO(n) __builtin_amdgcn_s_setprio(n)
constexpr unsigned F_EPOCH = 0, F_V = 1024, F_KV = F_V + 64 * 128, F_B1 = F_KV + 64 * 128, F_O = F_B1 + 192 * 128, F_P = F_O + 256 * 128, F_WT = F_P + 256 * 128;
constexpr unsigned F_R = 262144, WS_KV = F_R + 64 * 1024, WS_BT = WS_KV + 64 * KVT, WS_OF = WS_BT + 128 * 36 * 1024, WS_WT = WS_OF + 64 * 36 * 1024,
                   WS_P = WS_WT + 18 * 36 * 1024, WS_END = WS_P + 4 * SEQ * ROWB;

DI unsigned cvtpk(float lo, float hi) { f32x2 v = {lo, hi}; bf16x2 b = __builtin_convertvector(v, bf16x2); return __builtin_bit_cast(unsigned, b); }
DI float bflo(unsigned u) { return __uint_as_float(u << 16); }
DI float bfhi(unsigned u) { return __uint_as_float(u & 0xffff0000u); }
DI f32x16 mfma16(u32x4 a, u32x4 b, f32x16 c) { return __builtin_amdgcn_mfma_f32_32x32x16_bf16(__builtin_bit_cast(bf16x8, a), __builtin_bit_cast(bf16x8, b), c, 0, 0, 0); }
DI f32x16 mfma8(u32x2 a, u32x2 b, f32x16 c) { return __builtin_amdgcn_mfma_f32_32x32x8bf16_1k(__builtin_bit_cast(s16x4, a), __builtin_bit_cast(s16x4, b), c, 0, 0, 0); }
DI f32x16 smf(u32x4 a, f32x8 b, f32x16 c, int idx) { return __builtin_amdgcn_smfmac_f32_32x32x32_bf16(__builtin_bit_cast(bf16x8, a), __builtin_bit_cast(bf16x16, b), c, idx, 0, 0); }
DI f32x8 lo8(f32x16 x) { return __builtin_shufflevector(x, x, 0, 1, 2, 3, 4, 5, 6, 7); }
DI f32x8 hi8(f32x16 x) { return __builtin_shufflevector(x, x, 8, 9, 10, 11, 12, 13, 14, 15); }
DI float max3f(float a, float b, float c) { return __builtin_fmaxf(__builtin_fmaxf(a, b), c); }
DI rsrc_t mk_rsrc(const void* p, unsigned bytes) { return __builtin_amdgcn_make_buffer_rsrc((void*)p, 0, bytes, 0x00020000); }
DI u32x4 ld4c(rsrc_t r, unsigned v, unsigned s) { return __builtin_amdgcn_raw_buffer_load_b128(r, v, s, 16); }
DI unsigned ld1c(rsrc_t r, unsigned v, unsigned s) { return __builtin_amdgcn_raw_buffer_load_b32(r, v, s, 16); }
DI void st4c(u32x4 x, rsrc_t r, unsigned v, unsigned s) { __builtin_amdgcn_raw_buffer_store_b128(x, r, v + s, 0, 16); }
DI void st2c(u32x2 x, rsrc_t r, unsigned v, unsigned s) { __builtin_amdgcn_raw_buffer_store_b64(x, r, v + s, 0, 16); }
DI void st1c(unsigned x, rsrc_t r, unsigned v, unsigned s) { __builtin_amdgcn_raw_buffer_store_b32(x, r, v + s, 0, 16); }
DI void dma16(rsrc_t r, unsigned voff, unsigned soff, unsigned lds) {
  asm volatile("s_mov_b32 m0, %0\n\ts_nop 0\n\tbuffer_load_dwordx4 %1, %2, %3 offen lds" :: "s"(lds), "v"(voff), "s"(r), "s"(soff) : "memory");
}
DI void dma16c(rsrc_t r, unsigned voff, unsigned soff, unsigned lds) {
  asm volatile("s_mov_b32 m0, %0\n\ts_nop 0\n\tbuffer_load_dwordx4 %1, %2, %3 offen sc1 lds" :: "s"(lds), "v"(voff), "s"(r), "s"(soff) : "memory");
}
template <int N> DI void wait_bar() { asm volatile("s_waitcnt vmcnt(%0) lgkmcnt(0)\n\ts_barrier" :: "i"(N) : "memory"); }
DI void lds_bar() { asm volatile("s_waitcnt lgkmcnt(0)\n\ts_barrier" ::: "memory"); }
DI u32x4 lds16(unsigned a) { return *(const LAS u32x4*)(size_t)a; }
DI unsigned lds4(unsigned a) { return *(const LAS unsigned*)(size_t)a; }
typedef short v4i16_t __attribute__((ext_vector_type(4)));
DI u32x2 ldstr(unsigned a) { return __builtin_bit_cast(u32x2, __builtin_amdgcn_ds_read_tr16_b64_v4i16((LAS v4i16_t*)(size_t)a)); }
DI u32x4 pack8(float c0, float c1, float c2, float c3, float c4, float c5, float c6, float c7) {
  auto r0 = __builtin_amdgcn_permlane32_swap(cvtpk(c0, c1), cvtpk(c4, c5), false, false); auto r1 = __builtin_amdgcn_permlane32_swap(cvtpk(c2, c3), cvtpk(c6, c7), false, false);
  return (u32x4){r0[0], r1[0], r0[1], r1[1]};
}
DI u32x4 pack_pair(const f32x16& c, int p, float s) {
  return pack8(c[8 * p] * s, c[8 * p + 1] * s, c[8 * p + 2] * s, c[8 * p + 3] * s, c[8 * p + 4] * s, c[8 * p + 5] * s, c[8 * p + 6] * s, c[8 * p + 7] * s);
}

DI u64 ldflag(char* ws, unsigned off) { return __hip_atomic_load((GAS u64*)(ws + off), __ATOMIC_RELAXED, __HIP_MEMORY_SCOPE_AGENT); }
DI void stflag(char* ws, unsigned off, u64 v) { __hip_atomic_store((GAS u64*)(ws + off), v, __ATOMIC_RELAXED, __HIP_MEMORY_SCOPE_AGENT); }
DI u64 uni64(u64 v) { return ((u64)(unsigned)__builtin_amdgcn_readfirstlane((unsigned)(v >> 32)) << 32) | (u64)(unsigned)__builtin_amdgcn_readfirstlane((unsigned)v); }
template <bool F> DI void publish(char* ws, unsigned off, u64 tag) {
  asm volatile("s_waitcnt vmcnt(0) lgkmcnt(0)\n\ts_barrier" ::: "memory");
  if (F && threadIdx.x == 0) stflag(ws, off, tag);
}
template <int SL> DI void poll(char* ws, unsigned off, u64 tag) {
#pragma nounroll
  for (int spin = 0; spin < (1 << 17); ++spin) {
    if (__all(ldflag(ws, off) == tag)) break;
    __builtin_amdgcn_s_sleep(SL);
  }
  __builtin_amdgcn_fence(__ATOMIC_ACQUIRE, "wavefront");
}
template <bool F, int SL> DI void await(char* ws, unsigned off, u64 tag, int wid) {
  if (F && wid == 0) poll<SL>(ws, off, tag);
  asm volatile("s_waitcnt lgkmcnt(0)\n\ts_barrier" ::: "memory");
}

#define ATTN_LOOP_ASM \
  "s_movk_i32 %[cnt], 6\n\t" \
  ".p2align 6\n\t" \
  "1:\n\t" \
  "v_mfma_f32_32x32x16_bf16 v[80:95], v[128:131], v[108:111], 0\n\t" \
  "ds_read_b128 v[148:151], v158 offset:2560\n\t" \
  "v_exp_f32 v64, v64\n\t" \
  "v_exp_f32 v65, v65\n\t" \
  "v_exp_f32 v66, v66\n\t" \
  "v_exp_f32 v67, v67\n\t" \
  "v_mfma_f32_32x32x8_bf16 v[80:95], v[136:137], v[122:123], v[80:95]\n\t" \
  "s_cmp_lt_u32 %[sl], 40960\n\t" \
  "s_cselect_b32 %[dl], %[mb], %[mc]\n\t" \
  "v_exp_f32 v68, v68\n\t" \
  "v_exp_f32 v69, v69\n\t" \
  "v_exp_f32 v70, v70\n\t" \
  "v_exp_f32 v71, v71\n\t" \
  "v_smfmac_f32_32x32x32_bf16 v[48:63], v[152:155], v[96:103], v156\n\t" \
  "s_add_u32 m0, %[dl], %[sl]\n\t" \
  "s_setprio 3\n\t" \
  "v_exp_f32 v96, v72\n\t" \
  "v_exp_f32 v97, v73\n\t" \
  "v_exp_f32 v98, v74\n\t" \
  "v_exp_f32 v99, v75\n\t" \
  "v_smfmac_f32_32x32x32_bf16 v[0:15], v[140:143], v[64:71], v156\n\t" \
  "buffer_load_dwordx4 v160, %[rs], %[so] offen offset:0 sc1 lds\n\t" \
  "v_exp_f32 v100, v76\n\t" \
  "v_exp_f32 v101, v77\n\t" \
  "v_exp_f32 v102, v78\n\t" \
  "v_exp_f32 v103, v79\n\t" \
  "v_mfma_f32_32x32x16_bf16 v[64:79], v[128:131], v[112:115], 0\n\t" \
  "ds_read_b128 v[152:155], v158 offset:3200\n\t" \
  "buffer_load_dwordx4 v160, %[rs], %[so] offen offset:1024 sc1 lds\n\t" \
  "v_exp_f32 v80, v80\n\t" \
  "v_exp_f32 v81, v81\n\t" \
  "v_exp_f32 v82, v82\n\t" \
  "v_exp_f32 v83, v83\n\t" \
  "v_mfma_f32_32x32x8_bf16 v[64:79], v[136:137], v[124:125], v[64:79]\n\t" \
  "buffer_load_dwordx4 v160, %[rs], %[so] offen offset:2048 sc1 lds\n\t" \
  "v_exp_f32 v84, v84\n\t" \
  "v_exp_f32 v85, v85\n\t" \
  "v_exp_f32 v86, v86\n\t" \
  "v_exp_f32 v87, v87\n\t" \
  "v_smfmac_f32_32x32x32_bf16 v[0:15], v[144:147], v[96:103], v156\n\t" \
  "ds_read_b128 v[132:135], v157 offset:2560\n\t" \
  "buffer_load_dwordx4 v160, %[rs], %[so] offen offset:3072 sc1 lds\n\t" \
  "s_add_u32 m0, m0, 4096\n\t" \
  "v_exp_f32 v96, v88\n\t" \
  "v_exp_f32 v97, v89\n\t" \
  "v_exp_f32 v98, v90\n\t" \
  "v_exp_f32 v99, v91\n\t" \
  "v_smfmac_f32_32x32x32_bf16 v[16:31], v[140:143], v[80:87], v156\n\t" \
  "buffer_load_dwordx4 v161, %[rs], %[so] offen sc1 lds\n\t" \
  "s_setprio 1\n\t" \
  "s_add_u32 %[so], %[so], 10240\n\t" \
  "v_exp_f32 v100, v92\n\t" \
  "v_exp_f32 v101, v93\n\t" \
  "v_exp_f32 v102, v94\n\t" \
  "v_exp_f32 v103, v95\n\t" \
  "v_mfma_f32_32x32x16_bf16 v[80:95], v[128:131], v[116:119], 0\n\t" \
  "ds_read_b32 v138, v159 offset:2560\n\t" \
  "v_exp_f32 v64, v64\n\t" \
  "v_exp_f32 v65, v65\n\t" \
  "v_exp_f32 v66, v66\n\t" \
  "v_exp_f32 v67, v67\n\t" \
  "v_mfma_f32_32x32x8_bf16 v[80:95], v[136:137], v[126:127], v[80:95]\n\t" \
  "v_exp_f32 v68, v68\n\t" \
  "v_exp_f32 v69, v69\n\t" \
  "v_exp_f32 v70, v70\n\t" \
  "v_exp_f32 v71, v71\n\t" \
  "v_smfmac_f32_32x32x32_bf16 v[16:31], v[144:147], v[96:103], v156\n\t" \
  "v_exp_f32 v96, v72\n\t" \
  "v_exp_f32 v97, v73\n\t" \
  "v_exp_f32 v98, v74\n\t" \
  "v_exp_f32 v99, v75\n\t" \
  "v_smfmac_f32_32x32x32_bf16 v[32:47], v[140:143], v[64:71], v156\n\t" \
  "s_waitcnt lgkmcnt(0)\n\t" \
  "v_exp_f32 v100, v76\n\t" \
  "v_exp_f32 v101, v77\n\t" \
  "v_exp_f32 v102, v78\n\t" \
  "v_exp_f32 v103, v79\n\t" \
  "v_mfma_f32_32x32x16_bf16 v[64:79], v[132:135], v[104:107], 0\n\t" \
  "v_exp_f32 v80, v80\n\t" \
  "v_exp_f32 v81, v81\n\t" \
  "v_exp_f32 v82, v82\n\t" \
  "v_exp_f32 v83, v83\n\t" \
  "v_mfma_f32_32x32x8_bf16 v[64:79], v[138:139], v[120:121], v[64:79]\n\t" \
  "v_exp_f32 v84, v84\n\t" \
  "v_exp_f32 v85, v85\n\t" \
  "v_exp_f32 v86, v86\n\t" \
  "v_exp_f32 v87, v87\n\t" \
  "v_smfmac_f32_32x32x32_bf16 v[32:47], v[144:147], v[96:103], v156\n\t" \
  "v_exp_f32 v96, v88\n\t" \
  "v_exp_f32 v97, v89\n\t" \
  "v_exp_f32 v98, v90\n\t" \
  "v_exp_f32 v99, v91\n\t" \
  "v_smfmac_f32_32x32x32_bf16 v[48:63], v[140:143], v[80:87], v156\n\t" \
  "v_exp_f32 v100, v92\n\t" \
  "v_exp_f32 v101, v93\n\t" \
  "v_exp_f32 v102, v94\n\t" \
  "v_exp_f32 v103, v95\n\t" \
  "v_mfma_f32_32x32x16_bf16 v[80:95], v[132:135], v[108:111], 0\n\t" \
  "ds_read_b128 v[140:143], v158 offset:5120\n\t" \
  "v_exp_f32 v64, v64\n\t" \
  "v_exp_f32 v65, v65\n\t" \
  "v_exp_f32 v66, v66\n\t" \
  "v_exp_f32 v67, v67\n\t" \
  "v_mfma_f32_32x32x8_bf16 v[80:95], v[138:139], v[122:123], v[80:95]\n\t" \
  "v_exp_f32 v68, v68\n\t" \
  "v_exp_f32 v69, v69\n\t" \
  "v_exp_f32 v70, v70\n\t" \
  "v_exp_f32 v71, v71\n\t" \
  "v_smfmac_f32_32x32x32_bf16 v[48:63], v[144:147], v[96:103], v156\n\t" \
  "v_exp_f32 v96, v72\n\t" \
  "v_exp_f32 v97, v73\n\t" \
  "v_exp_f32 v98, v74\n\t" \
  "v_exp_f32 v99, v75\n\t" \
  "v_smfmac_f32_32x32x32_bf16 v[0:15], v[148:151], v[64:71], v156\n\t" \
  "v_exp_f32 v100, v76\n\t" \
  "v_exp_f32 v101, v77\n\t" \
  "v_exp_f32 v102, v78\n\t" \
  "v_exp_f32 v103, v79\n\t" \
  "v_mfma_f32_32x32x16_bf16 v[64:79], v[132:135], v[112:115], 0\n\t" \
  "ds_read_b128 v[144:147], v158 offset:5760\n\t" \
  "v_exp_f32 v80, v80\n\t" \
  "v_exp_f32 v81, v81\n\t" \
  "v_exp_f32 v82, v82\n\t" \
  "v_exp_f32 v83, v83\n\t" \
  "v_mfma_f32_32x32x8_bf16 v[64:79], v[138:139], v[124:125], v[64:79]\n\t" \
  "v_exp_f32 v84, v84\n\t" \
  "v_exp_f32 v85, v85\n\t" \
  "v_exp_f32 v86, v86\n\t" \
  "v_exp_f32 v87, v87\n\t" \
  "v_smfmac_f32_32x32x32_bf16 v[0:15], v[152:155], v[96:103], v156\n\t" \
  "ds_read_b128 v[128:131], v157 offset:5120\n\t" \
  "v_exp_f32 v96, v88\n\t" \
  "v_exp_f32 v97, v89\n\t" \
  "v_exp_f32 v98, v90\n\t" \
  "v_exp_f32 v99, v91\n\t" \
  "v_smfmac_f32_32x32x32_bf16 v[16:31], v[148:151], v[80:87], v156\n\t" \
  "v_exp_f32 v100, v92\n\t" \
  "v_exp_f32 v101, v93\n\t" \
  "v_exp_f32 v102, v94\n\t" \
  "v_exp_f32 v103, v95\n\t" \
  "v_mfma_f32_32x32x16_bf16 v[80:95], v[132:135], v[116:119], 0\n\t" \
  "ds_read_b32 v136, v159 offset:5120\n\t" \
  "v_exp_f32 v64, v64\n\t" \
  "v_exp_f32 v65, v65\n\t" \
  "v_exp_f32 v66, v66\n\t" \
  "v_exp_f32 v67, v67\n\t" \
  "v_mfma_f32_32x32x8_bf16 v[80:95], v[138:139], v[126:127], v[80:95]\n\t" \
  "v_exp_f32 v68, v68\n\t" \
  "v_exp_f32 v69, v69\n\t" \
  "v_exp_f32 v70, v70\n\t" \
  "v_exp_f32 v71, v71\n\t" \
  "v_smfmac_f32_32x32x32_bf16 v[16:31], v[152:155], v[96:103], v156\n\t" \
  "v_exp_f32 v96, v72\n\t" \
  "v_exp_f32 v97, v73\n\t" \
  "v_exp_f32 v98, v74\n\t" \
  "v_exp_f32 v99, v75\n\t" \
  "v_smfmac_f32_32x32x32_bf16 v[32:47], v[148:151], v[64:71], v156\n\t" \
  "s_waitcnt lgkmcnt(0)\n\t" \
  "v_exp_f32 v100, v76\n\t" \
  "v_exp_f32 v101, v77\n\t" \
  "v_exp_f32 v102, v78\n\t" \
  "v_exp_f32 v103, v79\n\t" \
  "v_mfma_f32_32x32x16_bf16 v[64:79], v[128:131], v[104:107], 0\n\t" \
  "v_exp_f32 v80, v80\n\t" \
  "v_exp_f32 v81, v81\n\t" \
  "v_exp_f32 v82, v82\n\t" \
  "v_exp_f32 v83, v83\n\t" \
  "v_mfma_f32_32x32x8_bf16 v[64:79], v[136:137], v[120:121], v[64:79]\n\t" \
  "v_exp_f32 v84, v84\n\t" \
  "v_exp_f32 v85, v85\n\t" \
  "v_exp_f32 v86, v86\n\t" \
  "v_exp_f32 v87, v87\n\t" \
  "v_smfmac_f32_32x32x32_bf16 v[32:47], v[152:155], v[96:103], v156\n\t" \
  "v_exp_f32 v96, v88\n\t" \
  "v_exp_f32 v97, v89\n\t" \
  "v_exp_f32 v98, v90\n\t" \
  "v_exp_f32 v99, v91\n\t" \
  "v_smfmac_f32_32x32x32_bf16 v[48:63], v[148:151], v[80:87], v156\n\t" \
  "v_exp_f32 v100, v92\n\t" \
  "v_exp_f32 v101, v93\n\t" \
  "v_exp_f32 v102, v94\n\t" \
  "v_exp_f32 v103, v95\n\t" \
  "v_mfma_f32_32x32x16_bf16 v[80:95], v[128:131], v[108:111], 0\n\t" \
  "ds_read_b128 v[148:151], v158 offset:7680\n\t" \
  "v_exp_f32 v64, v64\n\t" \
  "v_exp_f32 v65, v65\n\t" \
  "v_exp_f32 v66, v66\n\t" \
  "v_exp_f32 v67, v67\n\t" \
  "v_mfma_f32_32x32x8_bf16 v[80:95], v[136:137], v[122:123], v[80:95]\n\t" \
  "v_exp_f32 v68, v68\n\t" \
  "v_exp_f32 v69, v69\n\t" \
  "v_exp_f32 v70, v70\n\t" \
  "v_exp_f32 v71, v71\n\t" \
  "v_smfmac_f32_32x32x32_bf16 v[48:63], v[152:155], v[96:103], v156\n\t" \
  "v_exp_f32 v96, v72\n\t" \
  "v_exp_f32 v97, v73\n\t" \
  "v_exp_f32 v98, v74\n\t" \
  "v_exp_f32 v99, v75\n\t" \
  "v_smfmac_f32_32x32x32_bf16 v[0:15], v[140:143], v[64:71], v156\n\t" \
  "v_exp_f32 v100, v76\n\t" \
  "v_exp_f32 v101, v77\n\t" \
  "v_exp_f32 v102, v78\n\t" \
  "v_exp_f32 v103, v79\n\t" \
  "v_mfma_f32_32x32x16_bf16 v[64:79], v[128:131], v[112:115], 0\n\t" \
  "ds_read_b128 v[152:155], v158 offset:8320\n\t" \
  "v_exp_f32 v80, v80\n\t" \
  "v_exp_f32 v81, v81\n\t" \
  "v_exp_f32 v82, v82\n\t" \
  "v_exp_f32 v83, v83\n\t" \
  "v_mfma_f32_32x32x8_bf16 v[64:79], v[136:137], v[124:125], v[64:79]\n\t" \
  "v_exp_f32 v84, v84\n\t" \
  "v_exp_f32 v85, v85\n\t" \
  "v_exp_f32 v86, v86\n\t" \
  "v_exp_f32 v87, v87\n\t" \
  "v_smfmac_f32_32x32x32_bf16 v[0:15], v[144:147], v[96:103], v156\n\t" \
  "ds_read_b128 v[132:135], v157 offset:7680\n\t" \
  "v_exp_f32 v96, v88\n\t" \
  "v_exp_f32 v97, v89\n\t" \
  "v_exp_f32 v98, v90\n\t" \
  "v_exp_f32 v99, v91\n\t" \
  "v_smfmac_f32_32x32x32_bf16 v[16:31], v[140:143], v[80:87], v156\n\t" \
  "v_exp_f32 v100, v92\n\t" \
  "v_exp_f32 v101, v93\n\t" \
  "v_exp_f32 v102, v94\n\t" \
  "v_exp_f32 v103, v95\n\t" \
  "v_mfma_f32_32x32x16_bf16 v[80:95], v[128:131], v[116:119], 0\n\t" \
  "ds_read_b32 v138, v159 offset:7680\n\t" \
  "v_exp_f32 v64, v64\n\t" \
  "v_exp_f32 v65, v65\n\t" \
  "v_exp_f32 v66, v66\n\t" \
  "v_exp_f32 v67, v67\n\t" \
  "v_mfma_f32_32x32x8_bf16 v[80:95], v[136:137], v[126:127], v[80:95]\n\t" \
  "v_exp_f32 v68, v68\n\t" \
  "v_exp_f32 v69, v69\n\t" \
  "v_exp_f32 v70, v70\n\t" \
  "v_exp_f32 v71, v71\n\t" \
  "v_smfmac_f32_32x32x32_bf16 v[16:31], v[144:147], v[96:103], v156\n\t" \
  "v_exp_f32 v96, v72\n\t" \
  "v_exp_f32 v97, v73\n\t" \
  "v_exp_f32 v98, v74\n\t" \
  "v_exp_f32 v99, v75\n\t" \
  "v_smfmac_f32_32x32x32_bf16 v[32:47], v[140:143], v[64:71], v156\n\t" \
  "s_waitcnt lgkmcnt(0)\n\t" \
  "v_exp_f32 v100, v76\n\t" \
  "v_exp_f32 v101, v77\n\t" \
  "v_exp_f32 v102, v78\n\t" \
  "v_exp_f32 v103, v79\n\t" \
  "v_mfma_f32_32x32x16_bf16 v[64:79], v[132:135], v[104:107], 0\n\t" \
  "v_exp_f32 v80, v80\n\t" \
  "v_exp_f32 v81, v81\n\t" \
  "v_exp_f32 v82, v82\n\t" \
  "v_exp_f32 v83, v83\n\t" \
  "v_mfma_f32_32x32x8_bf16 v[64:79], v[138:139], v[120:121], v[64:79]\n\t" \
  "v_exp_f32 v84, v84\n\t" \
  "v_exp_f32 v85, v85\n\t" \
  "v_exp_f32 v86, v86\n\t" \
  "v_exp_f32 v87, v87\n\t" \
  "v_smfmac_f32_32x32x32_bf16 v[32:47], v[144:147], v[96:103], v156\n\t" \
  "v_exp_f32 v96, v88\n\t" \
  "v_exp_f32 v97, v89\n\t" \
  "v_exp_f32 v98, v90\n\t" \
  "v_exp_f32 v99, v91\n\t" \
  "v_smfmac_f32_32x32x32_bf16 v[48:63], v[140:143], v[80:87], v156\n\t" \
  "v_exp_f32 v100, v92\n\t" \
  "v_exp_f32 v101, v93\n\t" \
  "v_exp_f32 v102, v94\n\t" \
  "v_exp_f32 v103, v95\n\t" \
  "v_mfma_f32_32x32x16_bf16 v[80:95], v[132:135], v[108:111], 0\n\t" \
  "s_waitcnt vmcnt(5)\n\t" \
  "s_barrier\n\t" \
  "s_add_u32 %[sl], %[sl], 20480\n\t" \
  "s_cmp_eq_u32 %[sl], 81920\n\t" \
  "s_cselect_b32 %[dl], %[cn], 20480\n\t" \
  "s_cselect_b32 %[sl], 0, %[sl]\n\t" \
  "v_exp_f32 v64, v64\n\t" \
  "v_exp_f32 v65, v65\n\t" \
  "v_exp_f32 v66, v66\n\t" \
  "v_exp_f32 v67, v67\n\t" \
  "v_mfma_f32_32x32x8_bf16 v[80:95], v[138:139], v[122:123], v[80:95]\n\t" \
  "v_add_u32 v158, %[dl], v158\n\t" \
  "v_add_u32 v157, %[dl], v157\n\t" \
  "v_add_u32 v159, %[dl], v159\n\t" \
  "v_exp_f32 v68, v68\n\t" \
  "v_exp_f32 v69, v69\n\t" \
  "v_exp_f32 v70, v70\n\t" \
  "v_exp_f32 v71, v71\n\t" \
  "v_smfmac_f32_32x32x32_bf16 v[48:63], v[144:147], v[96:103], v156\n\t" \
  "ds_read_b128 v[140:143], v158 offset:0\n\t" \
  "v_exp_f32 v96, v72\n\t" \
  "v_exp_f32 v97, v73\n\t" \
  "v_exp_f32 v98, v74\n\t" \
  "v_exp_f32 v99, v75\n\t" \
  "v_smfmac_f32_32x32x32_bf16 v[0:15], v[148:151], v[64:71], v156\n\t" \
  "v_exp_f32 v100, v76\n\t" \
  "v_exp_f32 v101, v77\n\t" \
  "v_exp_f32 v102, v78\n\t" \
  "v_exp_f32 v103, v79\n\t" \
  "v_mfma_f32_32x32x16_bf16 v[64:79], v[132:135], v[112:115], 0\n\t" \
  "ds_read_b128 v[144:147], v158 offset:640\n\t" \
  "v_exp_f32 v80, v80\n\t" \
  "v_exp_f32 v81, v81\n\t" \
  "v_exp_f32 v82, v82\n\t" \
  "v_exp_f32 v83, v83\n\t" \
  "v_mfma_f32_32x32x8_bf16 v[64:79], v[138:139], v[124:125], v[64:79]\n\t" \
  "v_exp_f32 v84, v84\n\t" \
  "v_exp_f32 v85, v85\n\t" \
  "v_exp_f32 v86, v86\n\t" \
  "v_exp_f32 v87, v87\n\t" \
  "v_smfmac_f32_32x32x32_bf16 v[0:15], v[152:155], v[96:103], v156\n\t" \
  "ds_read_b128 v[128:131], v157 offset:0\n\t" \
  "v_exp_f32 v96, v88\n\t" \
  "v_exp_f32 v97, v89\n\t" \
  "v_exp_f32 v98, v90\n\t" \
  "v_exp_f32 v99, v91\n\t" \
  "v_smfmac_f32_32x32x32_bf16 v[16:31], v[148:151], v[80:87], v156\n\t" \
  "v_exp_f32 v100, v92\n\t" \
  "v_exp_f32 v101, v93\n\t" \
  "v_exp_f32 v102, v94\n\t" \
  "v_exp_f32 v103, v95\n\t" \
  "v_mfma_f32_32x32x16_bf16 v[80:95], v[132:135], v[116:119], 0\n\t" \
  "ds_read_b32 v136, v159 offset:0\n\t" \
  "v_exp_f32 v64, v64\n\t" \
  "v_exp_f32 v65, v65\n\t" \
  "v_exp_f32 v66, v66\n\t" \
  "v_exp_f32 v67, v67\n\t" \
  "v_mfma_f32_32x32x8_bf16 v[80:95], v[138:139], v[126:127], v[80:95]\n\t" \
  "v_exp_f32 v68, v68\n\t" \
  "v_exp_f32 v69, v69\n\t" \
  "v_exp_f32 v70, v70\n\t" \
  "v_exp_f32 v71, v71\n\t" \
  "v_smfmac_f32_32x32x32_bf16 v[16:31], v[152:155], v[96:103], v156\n\t" \
  "v_exp_f32 v96, v72\n\t" \
  "v_exp_f32 v97, v73\n\t" \
  "v_exp_f32 v98, v74\n\t" \
  "v_exp_f32 v99, v75\n\t" \
  "v_smfmac_f32_32x32x32_bf16 v[32:47], v[148:151], v[64:71], v156\n\t" \
  "s_waitcnt lgkmcnt(0)\n\t" \
  "v_exp_f32 v100, v76\n\t" \
  "v_exp_f32 v101, v77\n\t" \
  "v_exp_f32 v102, v78\n\t" \
  "v_exp_f32 v103, v79\n\t" \
  "v_mfma_f32_32x32x16_bf16 v[64:79], v[128:131], v[104:107], 0\n\t" \
  "v_exp_f32 v80, v80\n\t" \
  "v_exp_f32 v81, v81\n\t" \
  "v_exp_f32 v82, v82\n\t" \
  "v_exp_f32 v83, v83\n\t" \
  "v_mfma_f32_32x32x8_bf16 v[64:79], v[136:137], v[120:121], v[64:79]\n\t" \
  "v_exp_f32 v84, v84\n\t" \
  "v_exp_f32 v85, v85\n\t" \
  "v_exp_f32 v86, v86\n\t" \
  "v_exp_f32 v87, v87\n\t" \
  "v_smfmac_f32_32x32x32_bf16 v[32:47], v[152:155], v[96:103], v156\n\t" \
  "s_addk_i32 %[cnt], -1\n\t" \
  "v_exp_f32 v96, v88\n\t" \
  "v_exp_f32 v97, v89\n\t" \
  "v_exp_f32 v98, v90\n\t" \
  "v_exp_f32 v99, v91\n\t" \
  "v_smfmac_f32_32x32x32_bf16 v[48:63], v[148:151], v[80:87], v156\n\t" \
  "s_cmp_lg_u32 %[cnt], 0\n\t" \
  "v_exp_f32 v100, v92\n\t" \
  "v_exp_f32 v101, v93\n\t" \
  "v_exp_f32 v102, v94\n\t" \
  "v_exp_f32 v103, v95\n\t" \
  "s_cbranch_scc1 1b\n\t" \
  "s_movk_i32 %[cnt], 2\n\t" \
  ".p2align 6\n\t" \
  "2:\n\t" \
  "v_mfma_f32_32x32x16_bf16 v[80:95], v[128:131], v[108:111], 0\n\t" \
  "ds_read_b128 v[148:151], v158 offset:2560\n\t" \
  "v_exp_f32 v64, v64\n\t" \
  "v_exp_f32 v65, v65\n\t" \
  "v_exp_f32 v66, v66\n\t" \
  "v_exp_f32 v67, v67\n\t" \
  "v_mfma_f32_32x32x8_bf16 v[80:95], v[136:137], v[122:123], v[80:95]\n\t" \
  "v_exp_f32 v68, v68\n\t" \
  "v_exp_f32 v69, v69\n\t" \
  "v_exp_f32 v70, v70\n\t" \
  "v_exp_f32 v71, v71\n\t" \
  "v_smfmac_f32_32x32x32_bf16 v[48:63], v[152:155], v[96:103], v156\n\t" \
  "v_exp_f32 v96, v72\n\t" \
  "v_exp_f32 v97, v73\n\t" \
  "v_exp_f32 v98, v74\n\t" \
  "v_exp_f32 v99, v75\n\t" \
  "v_smfmac_f32_32x32x32_bf16 v[0:15], v[140:143], v[64:71], v156\n\t" \
  "v_exp_f32 v100, v76\n\t" \
  "v_exp_f32 v101, v77\n\t" \
  "v_exp_f32 v102, v78\n\t" \
  "v_exp_f32 v103, v79\n\t" \
  "v_mfma_f32_32x32x16_bf16 v[64:79], v[128:131], v[112:115], 0\n\t" \
  "ds_read_b128 v[152:155], v158 offset:3200\n\t" \
  "v_exp_f32 v80, v80\n\t" \
  "v_exp_f32 v81, v81\n\t" \
  "v_exp_f32 v82, v82\n\t" \
  "v_exp_f32 v83, v83\n\t" \
  "v_mfma_f32_32x32x8_bf16 v[64:79], v[136:137], v[124:125], v[64:79]\n\t" \
  "v_exp_f32 v84, v84\n\t" \
  "v_exp_f32 v85, v85\n\t" \
  "v_exp_f32 v86, v86\n\t" \
  "v_exp_f32 v87, v87\n\t" \
  "v_smfmac_f32_32x32x32_bf16 v[0:15], v[144:147], v[96:103], v156\n\t" \
  "ds_read_b128 v[132:135], v157 offset:2560\n\t" \
  "v_exp_f32 v96, v88\n\t" \
  "v_exp_f32 v97, v89\n\t" \
  "v_exp_f32 v98, v90\n\t" \
  "v_exp_f32 v99, v91\n\t" \
  "v_smfmac_f32_32x32x32_bf16 v[16:31], v[140:143], v[80:87], v156\n\t" \
  "v_exp_f32 v100, v92\n\t" \
  "v_exp_f32 v101, v93\n\t" \
  "v_exp_f32 v102, v94\n\t" \
  "v_exp_f32 v103, v95\n\t" \
  "v_mfma_f32_32x32x16_bf16 v[80:95], v[128:131], v[116:119], 0\n\t" \
  "ds_read_b32 v138, v159 offset:2560\n\t" \
  "v_exp_f32 v64, v64\n\t" \
  "v_exp_f32 v65, v65\n\t" \
  "v_exp_f32 v66, v66\n\t" \
  "v_exp_f32 v67, v67\n\t" \
  "v_mfma_f32_32x32x8_bf16 v[80:95], v[136:137], v[126:127], v[80:95]\n\t" \
  "v_exp_f32 v68, v68\n\t" \
  "v_exp_f32 v69, v69\n\t" \
  "v_exp_f32 v70, v70\n\t" \
  "v_exp_f32 v71, v71\n\t" \
  "v_smfmac_f32_32x32x32_bf16 v[16:31], v[144:147], v[96:103], v156\n\t" \
  "v_exp_f32 v96, v72\n\t" \
  "v_exp_f32 v97, v73\n\t" \
  "v_exp_f32 v98, v74\n\t" \
  "v_exp_f32 v99, v75\n\t" \
  "v_smfmac_f32_32x32x32_bf16 v[32:47], v[140:143], v[64:71], v156\n\t" \
  "s_waitcnt lgkmcnt(0)\n\t" \
  "v_exp_f32 v100, v76\n\t" \
  "v_exp_f32 v101, v77\n\t" \
  "v_exp_f32 v102, v78\n\t" \
  "v_exp_f32 v103, v79\n\t" \
  "v_mfma_f32_32x32x16_bf16 v[64:79], v[132:135], v[104:107], 0\n\t" \
  "v_exp_f32 v80, v80\n\t" \
  "v_exp_f32 v81, v81\n\t" \
  "v_exp_f32 v82, v82\n\t" \
  "v_exp_f32 v83, v83\n\t" \
  "v_mfma_f32_32x32x8_bf16 v[64:79], v[138:139], v[120:121], v[64:79]\n\t" \
  "v_exp_f32 v84, v84\n\t" \
  "v_exp_f32 v85, v85\n\t" \
  "v_exp_f32 v86, v86\n\t" \
  "v_exp_f32 v87, v87\n\t" \
  "v_smfmac_f32_32x32x32_bf16 v[32:47], v[144:147], v[96:103], v156\n\t" \
  "v_exp_f32 v96, v88\n\t" \
  "v_exp_f32 v97, v89\n\t" \
  "v_exp_f32 v98, v90\n\t" \
  "v_exp_f32 v99, v91\n\t" \
  "v_smfmac_f32_32x32x32_bf16 v[48:63], v[140:143], v[80:87], v156\n\t" \
  "v_exp_f32 v100, v92\n\t" \
  "v_exp_f32 v101, v93\n\t" \
  "v_exp_f32 v102, v94\n\t" \
  "v_exp_f32 v103, v95\n\t" \
  "v_mfma_f32_32x32x16_bf16 v[80:95], v[132:135], v[108:111], 0\n\t" \
  "ds_read_b128 v[140:143], v158 offset:5120\n\t" \
  "v_exp_f32 v64, v64\n\t" \
  "v_exp_f32 v65, v65\n\t" \
  "v_exp_f32 v66, v66\n\t" \
  "v_exp_f32 v67, v67\n\t" \
  "v_mfma_f32_32x32x8_bf16 v[80:95], v[138:139], v[122:123], v[80:95]\n\t" \
  "v_exp_f32 v68, v68\n\t" \
  "v_exp_f32 v69, v69\n\t" \
  "v_exp_f32 v70, v70\n\t" \
  "v_exp_f32 v71, v71\n\t" \
  "v_smfmac_f32_32x32x32_bf16 v[48:63], v[144:147], v[96:103], v156\n\t" \
  "v_exp_f32 v96, v72\n\t" \
  "v_exp_f32 v97, v73\n\t" \
  "v_exp_f32 v98, v74\n\t" \
  "v_exp_f32 v99, v75\n\t" \
  "v_smfmac_f32_32x32x32_bf16 v[0:15], v[148:151], v[64:71], v156\n\t" \
  "v_exp_f32 v100, v76\n\t" \
  "v_exp_f32 v101, v77\n\t" \
  "v_exp_f32 v102, v78\n\t" \
  "v_exp_f32 v103, v79\n\t" \
  "v_mfma_f32_32x32x16_bf16 v[64:79], v[132:135], v[112:115], 0\n\t" \
  "ds_read_b128 v[144:147], v158 offset:5760\n\t" \
  "v_exp_f32 v80, v80\n\t" \
  "v_exp_f32 v81, v81\n\t" \
  "v_exp_f32 v82, v82\n\t" \
  "v_exp_f32 v83, v83\n\t" \
  "v_mfma_f32_32x32x8_bf16 v[64:79], v[138:139], v[124:125], v[64:79]\n\t" \
  "v_exp_f32 v84, v84\n\t" \
  "v_exp_f32 v85, v85\n\t" \
  "v_exp_f32 v86, v86\n\t" \
  "v_exp_f32 v87, v87\n\t" \
  "v_smfmac_f32_32x32x32_bf16 v[0:15], v[152:155], v[96:103], v156\n\t" \
  "ds_read_b128 v[128:131], v157 offset:5120\n\t" \
  "v_exp_f32 v96, v88\n\t" \
  "v_exp_f32 v97, v89\n\t" \
  "v_exp_f32 v98, v90\n\t" \
  "v_exp_f32 v99, v91\n\t" \
  "v_smfmac_f32_32x32x32_bf16 v[16:31], v[148:151], v[80:87], v156\n\t" \
  "v_exp_f32 v100, v92\n\t" \
  "v_exp_f32 v101, v93\n\t" \
  "v_exp_f32 v102, v94\n\t" \
  "v_exp_f32 v103, v95\n\t" \
  "v_mfma_f32_32x32x16_bf16 v[80:95], v[132:135], v[116:119], 0\n\t" \
  "ds_read_b32 v136, v159 offset:5120\n\t" \
  "v_exp_f32 v64, v64\n\t" \
  "v_exp_f32 v65, v65\n\t" \
  "v_exp_f32 v66, v66\n\t" \
  "v_exp_f32 v67, v67\n\t" \
  "v_mfma_f32_32x32x8_bf16 v[80:95], v[138:139], v[126:127], v[80:95]\n\t" \
  "v_exp_f32 v68, v68\n\t" \
  "v_exp_f32 v69, v69\n\t" \
  "v_exp_f32 v70, v70\n\t" \
  "v_exp_f32 v71, v71\n\t" \
  "v_smfmac_f32_32x32x32_bf16 v[16:31], v[152:155], v[96:103], v156\n\t" \
  "v_exp_f32 v96, v72\n\t" \
  "v_exp_f32 v97, v73\n\t" \
  "v_exp_f32 v98, v74\n\t" \
  "v_exp_f32 v99, v75\n\t" \
  "v_smfmac_f32_32x32x32_bf16 v[32:47], v[148:151], v[64:71], v156\n\t" \
  "s_waitcnt lgkmcnt(0)\n\t" \
  "v_exp_f32 v100, v76\n\t" \
  "v_exp_f32 v101, v77\n\t" \
  "v_exp_f32 v102, v78\n\t" \
  "v_exp_f32 v103, v79\n\t" \
  "v_mfma_f32_32x32x16_bf16 v[64:79], v[128:131], v[104:107], 0\n\t" \
  "v_exp_f32 v80, v80\n\t" \
  "v_exp_f32 v81, v81\n\t" \
  "v_exp_f32 v82, v82\n\t" \
  "v_exp_f32 v83, v83\n\t" \
  "v_mfma_f32_32x32x8_bf16 v[64:79], v[136:137], v[120:121], v[64:79]\n\t" \
  "v_exp_f32 v84, v84\n\t" \
  "v_exp_f32 v85, v85\n\t" \
  "v_exp_f32 v86, v86\n\t" \
  "v_exp_f32 v87, v87\n\t" \
  "v_smfmac_f32_32x32x32_bf16 v[32:47], v[152:155], v[96:103], v156\n\t" \
  "v_exp_f32 v96, v88\n\t" \
  "v_exp_f32 v97, v89\n\t" \
  "v_exp_f32 v98, v90\n\t" \
  "v_exp_f32 v99, v91\n\t" \
  "v_smfmac_f32_32x32x32_bf16 v[48:63], v[148:151], v[80:87], v156\n\t" \
  "v_exp_f32 v100, v92\n\t" \
  "v_exp_f32 v101, v93\n\t" \
  "v_exp_f32 v102, v94\n\t" \
  "v_exp_f32 v103, v95\n\t" \
  "v_mfma_f32_32x32x16_bf16 v[80:95], v[128:131], v[108:111], 0\n\t" \
  "ds_read_b128 v[148:151], v158 offset:7680\n\t" \
  "v_exp_f32 v64, v64\n\t" \
  "v_exp_f32 v65, v65\n\t" \
  "v_exp_f32 v66, v66\n\t" \
  "v_exp_f32 v67, v67\n\t" \
  "v_mfma_f32_32x32x8_bf16 v[80:95], v[136:137], v[122:123], v[80:95]\n\t" \
  "v_exp_f32 v68, v68\n\t" \
  "v_exp_f32 v69, v69\n\t" \
  "v_exp_f32 v70, v70\n\t" \
  "v_exp_f32 v71, v71\n\t" \
  "v_smfmac_f32_32x32x32_bf16 v[48:63], v[152:155], v[96:103], v156\n\t" \
  "v_exp_f32 v96, v72\n\t" \
  "v_exp_f32 v97, v73\n\t" \
  "v_exp_f32 v98, v74\n\t" \
  "v_exp_f32 v99, v75\n\t" \
  "v_smfmac_f32_32x32x32_bf16 v[0:15], v[140:143], v[64:71], v156\n\t" \
  "v_exp_f32 v100, v76\n\t" \
  "v_exp_f32 v101, v77\n\t" \
  "v_exp_f32 v102, v78\n\t" \
  "v_exp_f32 v103, v79\n\t" \
  "v_mfma_f32_32x32x16_bf16 v[64:79], v[128:131], v[112:115], 0\n\t" \
  "ds_read_b128 v[152:155], v158 offset:8320\n\t" \
  "v_exp_f32 v80, v80\n\t" \
  "v_exp_f32 v81, v81\n\t" \
  "v_exp_f32 v82, v82\n\t" \
  "v_exp_f32 v83, v83\n\t" \
  "v_mfma_f32_32x32x8_bf16 v[64:79], v[136:137], v[124:125], v[64:79]\n\t" \
  "v_exp_f32 v84, v84\n\t" \
  "v_exp_f32 v85, v85\n\t" \
  "v_exp_f32 v86, v86\n\t" \
  "v_exp_f32 v87, v87\n\t" \
  "v_smfmac_f32_32x32x32_bf16 v[0:15], v[144:147], v[96:103], v156\n\t" \
  "ds_read_b128 v[132:135], v157 offset:7680\n\t" \
  "v_exp_f32 v96, v88\n\t" \
  "v_exp_f32 v97, v89\n\t" \
  "v_exp_f32 v98, v90\n\t" \
  "v_exp_f32 v99, v91\n\t" \
  "v_smfmac_f32_32x32x32_bf16 v[16:31], v[140:143], v[80:87], v156\n\t" \
  "v_exp_f32 v100, v92\n\t" \
  "v_exp_f32 v101, v93\n\t" \
  "v_exp_f32 v102, v94\n\t" \
  "v_exp_f32 v103, v95\n\t" \
  "v_mfma_f32_32x32x16_bf16 v[80:95], v[128:131], v[116:119], 0\n\t" \
  "ds_read_b32 v138, v159 offset:7680\n\t" \
  "v_exp_f32 v64, v64\n\t" \
  "v_exp_f32 v65, v65\n\t" \
  "v_exp_f32 v66, v66\n\t" \
  "v_exp_f32 v67, v67\n\t" \
  "v_mfma_f32_32x32x8_bf16 v[80:95], v[136:137], v[126:127], v[80:95]\n\t" \
  "v_exp_f32 v68, v68\n\t" \
  "v_exp_f32 v69, v69\n\t" \
  "v_exp_f32 v70, v70\n\t" \
  "v_exp_f32 v71, v71\n\t" \
  "v_smfmac_f32_32x32x32_bf16 v[16:31], v[144:147], v[96:103], v156\n\t" \
  "v_exp_f32 v96, v72\n\t" \
  "v_exp_f32 v97, v73\n\t" \
  "v_exp_f32 v98, v74\n\t" \
  "v_exp_f32 v99, v75\n\t" \
  "v_smfmac_f32_32x32x32_bf16 v[32:47], v[140:143], v[64:71], v156\n\t" \
  "s_waitcnt lgkmcnt(0)\n\t" \
  "v_exp_f32 v100, v76\n\t" \
  "v_exp_f32 v101, v77\n\t" \
  "v_exp_f32 v102, v78\n\t" \
  "v_exp_f32 v103, v79\n\t" \
  "v_mfma_f32_32x32x16_bf16 v[64:79], v[132:135], v[104:107], 0\n\t" \
  "v_exp_f32 v80, v80\n\t" \
  "v_exp_f32 v81, v81\n\t" \
  "v_exp_f32 v82, v82\n\t" \
  "v_exp_f32 v83, v83\n\t" \
  "v_mfma_f32_32x32x8_bf16 v[64:79], v[138:139], v[120:121], v[64:79]\n\t" \
  "v_exp_f32 v84, v84\n\t" \
  "v_exp_f32 v85, v85\n\t" \
  "v_exp_f32 v86, v86\n\t" \
  "v_exp_f32 v87, v87\n\t" \
  "v_smfmac_f32_32x32x32_bf16 v[32:47], v[144:147], v[96:103], v156\n\t" \
  "v_exp_f32 v96, v88\n\t" \
  "v_exp_f32 v97, v89\n\t" \
  "v_exp_f32 v98, v90\n\t" \
  "v_exp_f32 v99, v91\n\t" \
  "v_smfmac_f32_32x32x32_bf16 v[48:63], v[140:143], v[80:87], v156\n\t" \
  "v_exp_f32 v100, v92\n\t" \
  "v_exp_f32 v101, v93\n\t" \
  "v_exp_f32 v102, v94\n\t" \
  "v_exp_f32 v103, v95\n\t" \
  "v_mfma_f32_32x32x16_bf16 v[80:95], v[132:135], v[108:111], 0\n\t" \
  "s_waitcnt vmcnt(0)\n\t" \
  "s_barrier\n\t" \
  "s_add_u32 %[sl], %[sl], 20480\n\t" \
  "s_cmp_eq_u32 %[sl], 81920\n\t" \
  "s_cselect_b32 %[dl], %[cn], 20480\n\t" \
  "s_cselect_b32 %[sl], 0, %[sl]\n\t" \
  "v_exp_f32 v64, v64\n\t" \
  "v_exp_f32 v65, v65\n\t" \
  "v_exp_f32 v66, v66\n\t" \
  "v_exp_f32 v67, v67\n\t" \
  "v_mfma_f32_32x32x8_bf16 v[80:95], v[138:139], v[122:123], v[80:95]\n\t" \
  "v_add_u32 v158, %[dl], v158\n\t" \
  "v_add_u32 v157, %[dl], v157\n\t" \
  "v_add_u32 v159, %[dl], v159\n\t" \
  "v_exp_f32 v68, v68\n\t" \
  "v_exp_f32 v69, v69\n\t" \
  "v_exp_f32 v70, v70\n\t" \
  "v_exp_f32 v71, v71\n\t" \
  "v_smfmac_f32_32x32x32_bf16 v[48:63], v[144:147], v[96:103], v156\n\t" \
  "ds_read_b128 v[140:143], v158 offset:0\n\t" \
  "v_exp_f32 v96, v72\n\t" \
  "v_exp_f32 v97, v73\n\t" \
  "v_exp_f32 v98, v74\n\t" \
  "v_exp_f32 v99, v75\n\t" \
  "v_smfmac_f32_32x32x32_bf16 v[0:15], v[148:151], v[64:71], v156\n\t" \
  "v_exp_f32 v100, v76\n\t" \
  "v_exp_f32 v101, v77\n\t" \
  "v_exp_f32 v102, v78\n\t" \
  "v_exp_f32 v103, v79\n\t" \
  "v_mfma_f32_32x32x16_bf16 v[64:79], v[132:135], v[112:115], 0\n\t" \
  "ds_read_b128 v[144:147], v158 offset:640\n\t" \
  "v_exp_f32 v80, v80\n\t" \
  "v_exp_f32 v81, v81\n\t" \
  "v_exp_f32 v82, v82\n\t" \
  "v_exp_f32 v83, v83\n\t" \
  "v_mfma_f32_32x32x8_bf16 v[64:79], v[138:139], v[124:125], v[64:79]\n\t" \
  "v_exp_f32 v84, v84\n\t" \
  "v_exp_f32 v85, v85\n\t" \
  "v_exp_f32 v86, v86\n\t" \
  "v_exp_f32 v87, v87\n\t" \
  "v_smfmac_f32_32x32x32_bf16 v[0:15], v[152:155], v[96:103], v156\n\t" \
  "ds_read_b128 v[128:131], v157 offset:0\n\t" \
  "v_exp_f32 v96, v88\n\t" \
  "v_exp_f32 v97, v89\n\t" \
  "v_exp_f32 v98, v90\n\t" \
  "v_exp_f32 v99, v91\n\t" \
  "v_smfmac_f32_32x32x32_bf16 v[16:31], v[148:151], v[80:87], v156\n\t" \
  "v_exp_f32 v100, v92\n\t" \
  "v_exp_f32 v101, v93\n\t" \
  "v_exp_f32 v102, v94\n\t" \
  "v_exp_f32 v103, v95\n\t" \
  "v_mfma_f32_32x32x16_bf16 v[80:95], v[132:135], v[116:119], 0\n\t" \
  "ds_read_b32 v136, v159 offset:0\n\t" \
  "v_exp_f32 v64, v64\n\t" \
  "v_exp_f32 v65, v65\n\t" \
  "v_exp_f32 v66, v66\n\t" \
  "v_exp_f32 v67, v67\n\t" \
  "v_mfma_f32_32x32x8_bf16 v[80:95], v[138:139], v[126:127], v[80:95]\n\t" \
  "v_exp_f32 v68, v68\n\t" \
  "v_exp_f32 v69, v69\n\t" \
  "v_exp_f32 v70, v70\n\t" \
  "v_exp_f32 v71, v71\n\t" \
  "v_smfmac_f32_32x32x32_bf16 v[16:31], v[152:155], v[96:103], v156\n\t" \
  "v_exp_f32 v96, v72\n\t" \
  "v_exp_f32 v97, v73\n\t" \
  "v_exp_f32 v98, v74\n\t" \
  "v_exp_f32 v99, v75\n\t" \
  "v_smfmac_f32_32x32x32_bf16 v[32:47], v[148:151], v[64:71], v156\n\t" \
  "s_waitcnt lgkmcnt(0)\n\t" \
  "v_exp_f32 v100, v76\n\t" \
  "v_exp_f32 v101, v77\n\t" \
  "v_exp_f32 v102, v78\n\t" \
  "v_exp_f32 v103, v79\n\t" \
  "v_mfma_f32_32x32x16_bf16 v[64:79], v[128:131], v[104:107], 0\n\t" \
  "v_exp_f32 v80, v80\n\t" \
  "v_exp_f32 v81, v81\n\t" \
  "v_exp_f32 v82, v82\n\t" \
  "v_exp_f32 v83, v83\n\t" \
  "v_mfma_f32_32x32x8_bf16 v[64:79], v[136:137], v[120:121], v[64:79]\n\t" \
  "v_exp_f32 v84, v84\n\t" \
  "v_exp_f32 v85, v85\n\t" \
  "v_exp_f32 v86, v86\n\t" \
  "v_exp_f32 v87, v87\n\t" \
  "v_smfmac_f32_32x32x32_bf16 v[32:47], v[152:155], v[96:103], v156\n\t" \
  "s_addk_i32 %[cnt], -1\n\t" \
  "v_exp_f32 v96, v88\n\t" \
  "v_exp_f32 v97, v89\n\t" \
  "v_exp_f32 v98, v90\n\t" \
  "v_exp_f32 v99, v91\n\t" \
  "v_smfmac_f32_32x32x32_bf16 v[48:63], v[148:151], v[80:87], v156\n\t" \
  "s_cmp_lg_u32 %[cnt], 0\n\t" \
  "v_exp_f32 v100, v92\n\t" \
  "v_exp_f32 v101, v93\n\t" \
  "v_exp_f32 v102, v94\n\t" \
  "v_exp_f32 v103, v95\n\t" \
  "s_cbranch_scc1 2b\n\t" \
  "s_nop 1\n\t" \
  "v_smfmac_f32_32x32x32_bf16 v[48:63], v[152:155], v[96:103], v156\n\t" \
  "s_waitcnt vmcnt(0) lgkmcnt(0)\n\t" \
  "s_nop 15\n\t"

#define B2_LOOP_ASM \
  "s_add_u32 m0, %[mc], 0\n\t" \
  "s_nop 0\n\t" \
  "buffer_load_dwordx4 v176, %[rc], %[sc] offen offset:0 lds\n\t" \
  "buffer_load_dwordx4 v177, %[rc], %[sc] offen offset:1024 lds\n\t" \
  "buffer_load_dwordx4 v178, %[rc], %[sc] offen offset:2048 lds\n\t" \
  "buffer_load_dwordx4 v179, %[rc], %[sc] offen offset:3072 lds\n\t" \
  "s_add_u32 %[kc], %[kc], 128\n\t" \
  "s_and_b32 %[kc], %[kc], 2047\n\t" \
  "s_add_u32 %[sc], %[sc0], %[kc]\n\t" \
  "s_add_u32 m0, %[mb], 0\n\t" \
  "s_nop 0\n\t" \
  "buffer_load_dwordx4 v180, %[rb], %[sb] offen sc1 lds\n\t" \
  "s_add_u32 %[kb], %[kb], 64\n\t" \
  "buffer_load_dwordx4 v180, %[rb], %[sb] offen offset:1024 sc1 lds\n\t" \
  "s_and_b32 %[kb], %[kb], 2047\n\t" \
  "s_cmp_eq_u32 %[ex], 0\n\t" \
  "s_cbranch_scc1 11f\n\t" \
  "buffer_load_dwordx4 v180, %[rb], %[sb] offen offset:2048 sc1 lds\n\t" \
  "11:\n\t" \
  "s_mul_i32 %[sb], %[kb], 576\n\t" \
  "s_add_u32 %[sb], %[sb], %[sb0]\n\t" \
  "s_add_u32 m0, %[mb], 9216\n\t" \
  "s_nop 0\n\t" \
  "buffer_load_dwordx4 v180, %[rb], %[sb] offen sc1 lds\n\t" \
  "s_add_u32 %[kb], %[kb], 64\n\t" \
  "buffer_load_dwordx4 v180, %[rb], %[sb] offen offset:1024 sc1 lds\n\t" \
  "s_and_b32 %[kb], %[kb], 2047\n\t" \
  "s_cmp_eq_u32 %[ex], 0\n\t" \
  "s_cbranch_scc1 12f\n\t" \
  "buffer_load_dwordx4 v180, %[rb], %[sb] offen offset:2048 sc1 lds\n\t" \
  "12:\n\t" \
  "s_mul_i32 %[sb], %[kb], 576\n\t" \
  "s_add_u32 %[sb], %[sb], %[sb0]\n\t" \
  "s_add_u32 m0, %[mc], 16384\n\t" \
  "s_nop 0\n\t" \
  "buffer_load_dwordx4 v176, %[rc], %[sc] offen offset:0 lds\n\t" \
  "buffer_load_dwordx4 v177, %[rc], %[sc] offen offset:1024 lds\n\t" \
  "buffer_load_dwordx4 v178, %[rc], %[sc] offen offset:2048 lds\n\t" \
  "buffer_load_dwordx4 v179, %[rc], %[sc] offen offset:3072 lds\n\t" \
  "s_add_u32 %[kc], %[kc], 128\n\t" \
  "s_and_b32 %[kc], %[kc], 2047\n\t" \
  "s_add_u32 %[sc], %[sc0], %[kc]\n\t" \
  "s_add_u32 m0, %[mb], 18432\n\t" \
  "s_nop 0\n\t" \
  "buffer_load_dwordx4 v180, %[rb], %[sb] offen sc1 lds\n\t" \
  "s_add_u32 %[kb], %[kb], 64\n\t" \
  "buffer_load_dwordx4 v180, %[rb], %[sb] offen offset:1024 sc1 lds\n\t" \
  "s_and_b32 %[kb], %[kb], 2047\n\t" \
  "s_cmp_eq_u32 %[ex], 0\n\t" \
  "s_cbranch_scc1 13f\n\t" \
  "buffer_load_dwordx4 v180, %[rb], %[sb] offen offset:2048 sc1 lds\n\t" \
  "13:\n\t" \
  "s_mul_i32 %[sb], %[kb], 576\n\t" \
  "s_add_u32 %[sb], %[sb], %[sb0]\n\t" \
  "s_add_u32 m0, %[mb], 27648\n\t" \
  "s_nop 0\n\t" \
  "buffer_load_dwordx4 v180, %[rb], %[sb] offen sc1 lds\n\t" \
  "s_add_u32 %[kb], %[kb], 64\n\t" \
  "buffer_load_dwordx4 v180, %[rb], %[sb] offen offset:1024 sc1 lds\n\t" \
  "s_and_b32 %[kb], %[kb], 2047\n\t" \
  "s_cmp_eq_u32 %[ex], 0\n\t" \
  "s_cbranch_scc1 14f\n\t" \
  "buffer_load_dwordx4 v180, %[rb], %[sb] offen offset:2048 sc1 lds\n\t" \
  "14:\n\t" \
  "s_mul_i32 %[sb], %[kb], 576\n\t" \
  "s_add_u32 %[sb], %[sb], %[sb0]\n\t" \
  "s_waitcnt vmcnt(10)\n\t" \
  "s_barrier\n\t" \
  "ds_read_b128 v[72:75], v181 offset:0\n\t" \
  "ds_read_b128 v[76:79], v181 offset:2048\n\t" \
  "ds_read_b128 v[80:83], v182 offset:0\n\t" \
  "ds_read_b128 v[84:87], v182 offset:2048\n\t" \
  "ds_read_b128 v[104:107], v183 offset:0\n\t" \
  "ds_read_b128 v[108:111], v183 offset:1024\n\t" \
  "ds_read_b128 v[112:115], v183 offset:2048\n\t" \
  "ds_read_b128 v[116:119], v183 offset:3072\n\t" \
  "ds_read_b128 v[120:123], v183 offset:4096\n\t" \
  "ds_read_b128 v[124:127], v183 offset:5120\n\t" \
  "ds_read_b128 v[128:131], v183 offset:6144\n\t" \
  "ds_read_b128 v[132:135], v183 offset:7168\n\t" \
  "ds_read_b128 v[136:139], v183 offset:8192\n\t" \
  "s_movk_i32 %[cnt], 7\n\t" \
  ".p2align 6\n\t" \
  "1:\n\t" \
  "s_waitcnt vmcnt(8) lgkmcnt(0)\n\t" \
  "s_barrier\n\t" \
  "v_mfma_f32_16x16x32_bf16 v[0:3], v[104:107], v[72:75], v[0:3]\n\t" \
  "ds_read_b128 v[140:143], v183 offset:9216\n\t" \
  "ds_read_b128 v[144:147], v183 offset:10240\n\t" \
  "v_mfma_f32_16x16x32_bf16 v[4:7], v[104:107], v[76:79], v[4:7]\n\t" \
  "ds_read_b128 v[148:151], v183 offset:11264\n\t" \
  "ds_read_b128 v[152:155], v183 offset:12288\n\t" \
  "v_mfma_f32_16x16x32_bf16 v[8:11], v[108:111], v[72:75], v[8:11]\n\t" \
  "ds_read_b128 v[156:159], v183 offset:13312\n\t" \
  "v_mfma_f32_16x16x32_bf16 v[12:15], v[108:111], v[76:79], v[12:15]\n\t" \
  "ds_read_b128 v[160:163], v183 offset:14336\n\t" \
  "v_mfma_f32_16x16x32_bf16 v[16:19], v[112:115], v[72:75], v[16:19]\n\t" \
  "ds_read_b128 v[164:167], v183 offset:15360\n\t" \
  "v_mfma_f32_16x16x32_bf16 v[20:23], v[112:115], v[76:79], v[20:23]\n\t" \
  "ds_read_b128 v[168:171], v183 offset:16384\n\t" \
  "v_mfma_f32_16x16x32_bf16 v[24:27], v[116:119], v[72:75], v[24:27]\n\t" \
  "ds_read_b128 v[172:175], v183 offset:17408\n\t" \
  "v_mfma_f32_16x16x32_bf16 v[28:31], v[116:119], v[76:79], v[28:31]\n\t" \
  "s_add_u32 m0, %[mb], 0\n\t" \
  "v_mfma_f32_16x16x32_bf16 v[32:35], v[120:123], v[72:75], v[32:35]\n\t" \
  "buffer_load_dwordx4 v180, %[rb], %[sb] offen sc1 lds\n\t" \
  "s_add_u32 %[kb], %[kb], 64\n\t" \
  "v_mfma_f32_16x16x32_bf16 v[36:39], v[120:123], v[76:79], v[36:39]\n\t" \
  "buffer_load_dwordx4 v180, %[rb], %[sb] offen offset:1024 sc1 lds\n\t" \
  "s_and_b32 %[kb], %[kb], 2047\n\t" \
  "v_mfma_f32_16x16x32_bf16 v[40:43], v[124:127], v[72:75], v[40:43]\n\t" \
  "s_cmp_eq_u32 %[ex], 0\n\t" \
  "s_cbranch_scc1 15f\n\t" \
  "buffer_load_dwordx4 v180, %[rb], %[sb] offen offset:2048 sc1 lds\n\t" \
  "15:\n\t" \
  "v_mfma_f32_16x16x32_bf16 v[44:47], v[124:127], v[76:79], v[44:47]\n\t" \
  "s_mul_i32 %[sb], %[kb], 576\n\t" \
  "s_add_u32 %[sb], %[sb], %[sb0]\n\t" \
  "v_mfma_f32_16x16x32_bf16 v[48:51], v[128:131], v[72:75], v[48:51]\n\t" \
  "s_add_u32 m0, %[mc], 0\n\t" \
  "v_mfma_f32_16x16x32_bf16 v[52:55], v[128:131], v[76:79], v[52:55]\n\t" \
  "buffer_load_dwordx4 v176, %[rc], %[sc] offen offset:0 lds\n\t" \
  "v_mfma_f32_16x16x32_bf16 v[56:59], v[132:135], v[72:75], v[56:59]\n\t" \
  "buffer_load_dwordx4 v177, %[rc], %[sc] offen offset:1024 lds\n\t" \
  "v_mfma_f32_16x16x32_bf16 v[60:63], v[132:135], v[76:79], v[60:63]\n\t" \
  "buffer_load_dwordx4 v178, %[rc], %[sc] offen offset:2048 lds\n\t" \
  "v_mfma_f32_16x16x32_bf16 v[64:67], v[136:139], v[72:75], v[64:67]\n\t" \
  "buffer_load_dwordx4 v179, %[rc], %[sc] offen offset:3072 lds\n\t" \
  "v_mfma_f32_16x16x32_bf16 v[68:71], v[136:139], v[76:79], v[68:71]\n\t" \
  "s_add_u32 %[kc], %[kc], 128\n\t" \
  "s_and_b32 %[kc], %[kc], 2047\n\t" \
  "s_add_u32 %[sc], %[sc0], %[kc]\n\t" \
  "s_waitcnt vmcnt(8) lgkmcnt(0)\n\t" \
  "s_barrier\n\t" \
  "v_mfma_f32_16x16x32_bf16 v[0:3], v[140:143], v[80:83], v[0:3]\n\t" \
  "ds_read_b128 v[104:107], v183 offset:18432\n\t" \
  "v_mfma_f32_16x16x32_bf16 v[4:7], v[140:143], v[84:87], v[4:7]\n\t" \
  "ds_read_b128 v[108:111], v183 offset:19456\n\t" \
  "v_mfma_f32_16x16x32_bf16 v[8:11], v[144:147], v[80:83], v[8:11]\n\t" \
  "ds_read_b128 v[112:115], v183 offset:20480\n\t" \
  "v_mfma_f32_16x16x32_bf16 v[12:15], v[144:147], v[84:87], v[12:15]\n\t" \
  "ds_read_b128 v[116:119], v183 offset:21504\n\t" \
  "v_mfma_f32_16x16x32_bf16 v[16:19], v[148:151], v[80:83], v[16:19]\n\t" \
  "ds_read_b128 v[120:123], v183 offset:22528\n\t" \
  "v_mfma_f32_16x16x32_bf16 v[20:23], v[148:151], v[84:87], v[20:23]\n\t" \
  "ds_read_b128 v[124:127], v183 offset:23552\n\t" \
  "v_mfma_f32_16x16x32_bf16 v[24:27], v[152:155], v[80:83], v[24:27]\n\t" \
  "ds_read_b128 v[128:131], v183 offset:24576\n\t" \
  "v_mfma_f32_16x16x32_bf16 v[28:31], v[152:155], v[84:87], v[28:31]\n\t" \
  "ds_read_b128 v[132:135], v183 offset:25600\n\t" \
  "v_mfma_f32_16x16x32_bf16 v[32:35], v[156:159], v[80:83], v[32:35]\n\t" \
  "ds_read_b128 v[136:139], v183 offset:26624\n\t" \
  "v_mfma_f32_16x16x32_bf16 v[36:39], v[156:159], v[84:87], v[36:39]\n\t" \
  "ds_read_b128 v[88:91], v181 offset:16384\n\t" \
  "v_mfma_f32_16x16x32_bf16 v[40:43], v[160:163], v[80:83], v[40:43]\n\t" \
  "ds_read_b128 v[92:95], v181 offset:18432\n\t" \
  "v_mfma_f32_16x16x32_bf16 v[44:47], v[160:163], v[84:87], v[44:47]\n\t" \
  "ds_read_b128 v[96:99], v182 offset:16384\n\t" \
  "v_mfma_f32_16x16x32_bf16 v[48:51], v[164:167], v[80:83], v[48:51]\n\t" \
  "ds_read_b128 v[100:103], v182 offset:18432\n\t" \
  "v_mfma_f32_16x16x32_bf16 v[52:55], v[164:167], v[84:87], v[52:55]\n\t" \
  "s_add_u32 m0, %[mb], 9216\n\t" \
  "v_mfma_f32_16x16x32_bf16 v[56:59], v[168:171], v[80:83], v[56:59]\n\t" \
  "buffer_load_dwordx4 v180, %[rb], %[sb] offen sc1 lds\n\t" \
  "s_add_u32 %[kb], %[kb], 64\n\t" \
  "v_mfma_f32_16x16x32_bf16 v[60:63], v[168:171], v[84:87], v[60:63]\n\t" \
  "buffer_load_dwordx4 v180, %[rb], %[sb] offen offset:1024 sc1 lds\n\t" \
  "s_and_b32 %[kb], %[kb], 2047\n\t" \
  "v_mfma_f32_16x16x32_bf16 v[64:67], v[172:175], v[80:83], v[64:67]\n\t" \
  "s_cmp_eq_u32 %[ex], 0\n\t" \
  "s_cbranch_scc1 16f\n\t" \
  "buffer_load_dwordx4 v180, %[rb], %[sb] offen offset:2048 sc1 lds\n\t" \
  "16:\n\t" \
  "v_mfma_f32_16x16x32_bf16 v[68:71], v[172:175], v[84:87], v[68:71]\n\t" \
  "s_mul_i32 %[sb], %[kb], 576\n\t" \
  "s_add_u32 %[sb], %[sb], %[sb0]\n\t" \
  "s_waitcnt vmcnt(8) lgkmcnt(0)\n\t" \
  "s_barrier\n\t" \
  "v_mfma_f32_16x16x32_bf16 v[0:3], v[104:107], v[88:91], v[0:3]\n\t" \
  "ds_read_b128 v[140:143], v183 offset:27648\n\t" \
  "ds_read_b128 v[144:147], v183 offset:28672\n\t" \
  "v_mfma_f32_16x16x32_bf16 v[4:7], v[104:107], v[92:95], v[4:7]\n\t" \
  "ds_read_b128 v[148:151], v183 offset:29696\n\t" \
  "ds_read_b128 v[152:155], v183 offset:30720\n\t" \
  "v_mfma_f32_16x16x32_bf16 v[8:11], v[108:111], v[88:91], v[8:11]\n\t" \
  "ds_read_b128 v[156:159], v183 offset:31744\n\t" \
  "v_mfma_f32_16x16x32_bf16 v[12:15], v[108:111], v[92:95], v[12:15]\n\t" \
  "ds_read_b128 v[160:163], v183 offset:32768\n\t" \
  "v_mfma_f32_16x16x32_bf16 v[16:19], v[112:115], v[88:91], v[16:19]\n\t" \
  "ds_read_b128 v[164:167], v183 offset:33792\n\t" \
  "v_mfma_f32_16x16x32_bf16 v[20:23], v[112:115], v[92:95], v[20:23]\n\t" \
  "ds_read_b128 v[168:171], v183 offset:34816\n\t" \
  "v_mfma_f32_16x16x32_bf16 v[24:27], v[116:119], v[88:91], v[24:27]\n\t" \
  "ds_read_b128 v[172:175], v183 offset:35840\n\t" \
  "v_mfma_f32_16x16x32_bf16 v[28:31], v[116:119], v[92:95], v[28:31]\n\t" \
  "s_add_u32 m0, %[mb], 18432\n\t" \
  "v_mfma_f32_16x16x32_bf16 v[32:35], v[120:123], v[88:91], v[32:35]\n\t" \
  "buffer_load_dwordx4 v180, %[rb], %[sb] offen sc1 lds\n\t" \
  "s_add_u32 %[kb], %[kb], 64\n\t" \
  "v_mfma_f32_16x16x32_bf16 v[36:39], v[120:123], v[92:95], v[36:39]\n\t" \
  "buffer_load_dwordx4 v180, %[rb], %[sb] offen offset:1024 sc1 lds\n\t" \
  "s_and_b32 %[kb], %[kb], 2047\n\t" \
  "v_mfma_f32_16x16x32_bf16 v[40:43], v[124:127], v[88:91], v[40:43]\n\t" \
  "s_cmp_eq_u32 %[ex], 0\n\t" \
  "s_cbranch_scc1 17f\n\t" \
  "buffer_load_dwordx4 v180, %[rb], %[sb] offen offset:2048 sc1 lds\n\t" \
  "17:\n\t" \
  "v_mfma_f32_16x16x32_bf16 v[44:47], v[124:127], v[92:95], v[44:47]\n\t" \
  "s_mul_i32 %[sb], %[kb], 576\n\t" \
  "s_add_u32 %[sb], %[sb], %[sb0]\n\t" \
  "v_mfma_f32_16x16x32_bf16 v[48:51], v[128:131], v[88:91], v[48:51]\n\t" \
  "s_add_u32 m0, %[mc], 16384\n\t" \
  "v_mfma_f32_16x16x32_bf16 v[52:55], v[128:131], v[92:95], v[52:55]\n\t" \
  "buffer_load_dwordx4 v176, %[rc], %[sc] offen offset:0 lds\n\t" \
  "v_mfma_f32_16x16x32_bf16 v[56:59], v[132:135], v[88:91], v[56:59]\n\t" \
  "buffer_load_dwordx4 v177, %[rc], %[sc] offen offset:1024 lds\n\t" \
  "v_mfma_f32_16x16x32_bf16 v[60:63], v[132:135], v[92:95], v[60:63]\n\t" \
  "buffer_load_dwordx4 v178, %[rc], %[sc] offen offset:2048 lds\n\t" \
  "v_mfma_f32_16x16x32_bf16 v[64:67], v[136:139], v[88:91], v[64:67]\n\t" \
  "buffer_load_dwordx4 v179, %[rc], %[sc] offen offset:3072 lds\n\t" \
  "v_mfma_f32_16x16x32_bf16 v[68:71], v[136:139], v[92:95], v[68:71]\n\t" \
  "s_add_u32 %[kc], %[kc], 128\n\t" \
  "s_and_b32 %[kc], %[kc], 2047\n\t" \
  "s_add_u32 %[sc], %[sc0], %[kc]\n\t" \
  "s_waitcnt vmcnt(8) lgkmcnt(0)\n\t" \
  "s_barrier\n\t" \
  "v_mfma_f32_16x16x32_bf16 v[0:3], v[140:143], v[96:99], v[0:3]\n\t" \
  "ds_read_b128 v[104:107], v183 offset:0\n\t" \
  "v_mfma_f32_16x16x32_bf16 v[4:7], v[140:143], v[100:103], v[4:7]\n\t" \
  "ds_read_b128 v[108:111], v183 offset:1024\n\t" \
  "v_mfma_f32_16x16x32_bf16 v[8:11], v[144:147], v[96:99], v[8:11]\n\t" \
  "ds_read_b128 v[112:115], v183 offset:2048\n\t" \
  "v_mfma_f32_16x16x32_bf16 v[12:15], v[144:147], v[100:103], v[12:15]\n\t" \
  "ds_read_b128 v[116:119], v183 offset:3072\n\t" \
  "v_mfma_f32_16x16x32_bf16 v[16:19], v[148:151], v[96:99], v[16:19]\n\t" \
  "ds_read_b128 v[120:123], v183 offset:4096\n\t" \
  "v_mfma_f32_16x16x32_bf16 v[20:23], v[148:151], v[100:103], v[20:23]\n\t" \
  "ds_read_b128 v[124:127], v183 offset:5120\n\t" \
  "v_mfma_f32_16x16x32_bf16 v[24:27], v[152:155], v[96:99], v[24:27]\n\t" \
  "ds_read_b128 v[128:131], v183 offset:6144\n\t" \
  "v_mfma_f32_16x16x32_bf16 v[28:31], v[152:155], v[100:103], v[28:31]\n\t" \
  "ds_read_b128 v[132:135], v183 offset:7168\n\t" \
  "v_mfma_f32_16x16x32_bf16 v[32:35], v[156:159], v[96:99], v[32:35]\n\t" \
  "ds_read_b128 v[136:139], v183 offset:8192\n\t" \
  "v_mfma_f32_16x16x32_bf16 v[36:39], v[156:159], v[100:103], v[36:39]\n\t" \
  "ds_read_b128 v[72:75], v181 offset:0\n\t" \
  "v_mfma_f32_16x16x32_bf16 v[40:43], v[160:163], v[96:99], v[40:43]\n\t" \
  "ds_read_b128 v[76:79], v181 offset:2048\n\t" \
  "v_mfma_f32_16x16x32_bf16 v[44:47], v[160:163], v[100:103], v[44:47]\n\t" \
  "ds_read_b128 v[80:83], v182 offset:0\n\t" \
  "v_mfma_f32_16x16x32_bf16 v[48:51], v[164:167], v[96:99], v[48:51]\n\t" \
  "ds_read_b128 v[84:87], v182 offset:2048\n\t" \
  "v_mfma_f32_16x16x32_bf16 v[52:55], v[164:167], v[100:103], v[52:55]\n\t" \
  "s_add_u32 m0, %[mb], 27648\n\t" \
  "v_mfma_f32_16x16x32_bf16 v[56:59], v[168:171], v[96:99], v[56:59]\n\t" \
  "buffer_load_dwordx4 v180, %[rb], %[sb] offen sc1 lds\n\t" \
  "s_add_u32 %[kb], %[kb], 64\n\t" \
  "v_mfma_f32_16x16x32_bf16 v[60:63], v[168:171], v[100:103], v[60:63]\n\t" \
  "buffer_load_dwordx4 v180, %[rb], %[sb] offen offset:1024 sc1 lds\n\t" \
  "s_and_b32 %[kb], %[kb], 2047\n\t" \
  "v_mfma_f32_16x16x32_bf16 v[64:67], v[172:175], v[96:99], v[64:67]\n\t" \
  "s_cmp_eq_u32 %[ex], 0\n\t" \
  "s_cbranch_scc1 18f\n\t" \
  "buffer_load_dwordx4 v180, %[rb], %[sb] offen offset:2048 sc1 lds\n\t" \
  "18:\n\t" \
  "v_mfma_f32_16x16x32_bf16 v[68:71], v[172:175], v[100:103], v[68:71]\n\t" \
  "s_mul_i32 %[sb], %[kb], 576\n\t" \
  "s_add_u32 %[sb], %[sb], %[sb0]\n\t" \
  "s_addk_i32 %[cnt], -1\n\t" \
  "s_cmp_lg_u32 %[cnt], 0\n\t" \
  "s_cbranch_scc1 1b\n\t" \
  "s_waitcnt vmcnt(8) lgkmcnt(0)\n\t" \
  "s_barrier\n\t" \
  "v_mfma_f32_16x16x32_bf16 v[0:3], v[104:107], v[72:75], v[0:3]\n\t" \
  "ds_read_b128 v[140:143], v183 offset:9216\n\t" \
  "v_mfma_f32_16x16x32_bf16 v[4:7], v[104:107], v[76:79], v[4:7]\n\t" \
  "ds_read_b128 v[144:147], v183 offset:10240\n\t" \
  "v_mfma_f32_16x16x32_bf16 v[8:11], v[108:111], v[72:75], v[8:11]\n\t" \
  "ds_read_b128 v[148:151], v183 offset:11264\n\t" \
  "v_mfma_f32_16x16x32_bf16 v[12:15], v[108:111], v[76:79], v[12:15]\n\t" \
  "ds_read_b128 v[152:155], v183 offset:12288\n\t" \
  "v_mfma_f32_16x16x32_bf16 v[16:19], v[112:115], v[72:75], v[16:19]\n\t" \
  "ds_read_b128 v[156:159], v183 offset:13312\n\t" \
  "v_mfma_f32_16x16x32_bf16 v[20:23], v[112:115], v[76:79], v[20:23]\n\t" \
  "ds_read_b128 v[160:163], v183 offset:14336\n\t" \
  "v_mfma_f32_16x16x32_bf16 v[24:27], v[116:119], v[72:75], v[24:27]\n\t" \
  "ds_read_b128 v[164:167], v183 offset:15360\n\t" \
  "v_mfma_f32_16x16x32_bf16 v[28:31], v[116:119], v[76:79], v[28:31]\n\t" \
  "ds_read_b128 v[168:171], v183 offset:16384\n\t" \
  "v_mfma_f32_16x16x32_bf16 v[32:35], v[120:123], v[72:75], v[32:35]\n\t" \
  "ds_read_b128 v[172:175], v183 offset:17408\n\t" \
  "v_mfma_f32_16x16x32_bf16 v[36:39], v[120:123], v[76:79], v[36:39]\n\t" \
  "v_mfma_f32_16x16x32_bf16 v[40:43], v[124:127], v[72:75], v[40:43]\n\t" \
  "v_mfma_f32_16x16x32_bf16 v[44:47], v[124:127], v[76:79], v[44:47]\n\t" \
  "v_mfma_f32_16x16x32_bf16 v[48:51], v[128:131], v[72:75], v[48:51]\n\t" \
  "v_mfma_f32_16x16x32_bf16 v[52:55], v[128:131], v[76:79], v[52:55]\n\t" \
  "v_mfma_f32_16x16x32_bf16 v[56:59], v[132:135], v[72:75], v[56:59]\n\t" \
  "v_mfma_f32_16x16x32_bf16 v[60:63], v[132:135], v[76:79], v[60:63]\n\t" \
  "v_mfma_f32_16x16x32_bf16 v[64:67], v[136:139], v[72:75], v[64:67]\n\t" \
  "v_mfma_f32_16x16x32_bf16 v[68:71], v[136:139], v[76:79], v[68:71]\n\t" \
  "s_waitcnt vmcnt(2) lgkmcnt(0)\n\t" \
  "s_barrier\n\t" \
  "v_mfma_f32_16x16x32_bf16 v[0:3], v[140:143], v[80:83], v[0:3]\n\t" \
  "ds_read_b128 v[104:107], v183 offset:18432\n\t" \
  "v_mfma_f32_16x16x32_bf16 v[4:7], v[140:143], v[84:87], v[4:7]\n\t" \
  "ds_read_b128 v[108:111], v183 offset:19456\n\t" \
  "v_mfma_f32_16x16x32_bf16 v[8:11], v[144:147], v[80:83], v[8:11]\n\t" \
  "ds_read_b128 v[112:115], v183 offset:20480\n\t" \
  "v_mfma_f32_16x16x32_bf16 v[12:15], v[144:147], v[84:87], v[12:15]\n\t" \
  "ds_read_b128 v[116:119], v183 offset:21504\n\t" \
  "v_mfma_f32_16x16x32_bf16 v[16:19], v[148:151], v[80:83], v[16:19]\n\t" \
  "ds_read_b128 v[120:123], v183 offset:22528\n\t" \
  "v_mfma_f32_16x16x32_bf16 v[20:23], v[148:151], v[84:87], v[20:23]\n\t" \
  "ds_read_b128 v[124:127], v183 offset:23552\n\t" \
  "v_mfma_f32_16x16x32_bf16 v[24:27], v[152:155], v[80:83], v[24:27]\n\t" \
  "ds_read_b128 v[128:131], v183 offset:24576\n\t" \
  "v_mfma_f32_16x16x32_bf16 v[28:31], v[152:155], v[84:87], v[28:31]\n\t" \
  "ds_read_b128 v[132:135], v183 offset:25600\n\t" \
  "v_mfma_f32_16x16x32_bf16 v[32:35], v[156:159], v[80:83], v[32:35]\n\t" \
  "ds_read_b128 v[136:139], v183 offset:26624\n\t" \
  "v_mfma_f32_16x16x32_bf16 v[36:39], v[156:159], v[84:87], v[36:39]\n\t" \
  "ds_read_b128 v[88:91], v181 offset:16384\n\t" \
  "v_mfma_f32_16x16x32_bf16 v[40:43], v[160:163], v[80:83], v[40:43]\n\t" \
  "ds_read_b128 v[92:95], v181 offset:18432\n\t" \
  "v_mfma_f32_16x16x32_bf16 v[44:47], v[160:163], v[84:87], v[44:47]\n\t" \
  "ds_read_b128 v[96:99], v182 offset:16384\n\t" \
  "v_mfma_f32_16x16x32_bf16 v[48:51], v[164:167], v[80:83], v[48:51]\n\t" \
  "ds_read_b128 v[100:103], v182 offset:18432\n\t" \
  "v_mfma_f32_16x16x32_bf16 v[52:55], v[164:167], v[84:87], v[52:55]\n\t" \
  "v_mfma_f32_16x16x32_bf16 v[56:59], v[168:171], v[80:83], v[56:59]\n\t" \
  "v_mfma_f32_16x16x32_bf16 v[60:63], v[168:171], v[84:87], v[60:63]\n\t" \
  "v_mfma_f32_16x16x32_bf16 v[64:67], v[172:175], v[80:83], v[64:67]\n\t" \
  "v_mfma_f32_16x16x32_bf16 v[68:71], v[172:175], v[84:87], v[68:71]\n\t" \
  "s_waitcnt vmcnt(0) lgkmcnt(0)\n\t" \
  "s_barrier\n\t" \
  "v_mfma_f32_16x16x32_bf16 v[0:3], v[104:107], v[88:91], v[0:3]\n\t" \
  "ds_read_b128 v[140:143], v183 offset:27648\n\t" \
  "v_mfma_f32_16x16x32_bf16 v[4:7], v[104:107], v[92:95], v[4:7]\n\t" \
  "ds_read_b128 v[144:147], v183 offset:28672\n\t" \
  "v_mfma_f32_16x16x32_bf16 v[8:11], v[108:111], v[88:91], v[8:11]\n\t" \
  "ds_read_b128 v[148:151], v183 offset:29696\n\t" \
  "v_mfma_f32_16x16x32_bf16 v[12:15], v[108:111], v[92:95], v[12:15]\n\t" \
  "ds_read_b128 v[152:155], v183 offset:30720\n\t" \
  "v_mfma_f32_16x16x32_bf16 v[16:19], v[112:115], v[88:91], v[16:19]\n\t" \
  "ds_read_b128 v[156:159], v183 offset:31744\n\t" \
  "v_mfma_f32_16x16x32_bf16 v[20:23], v[112:115], v[92:95], v[20:23]\n\t" \
  "ds_read_b128 v[160:163], v183 offset:32768\n\t" \
  "v_mfma_f32_16x16x32_bf16 v[24:27], v[116:119], v[88:91], v[24:27]\n\t" \
  "ds_read_b128 v[164:167], v183 offset:33792\n\t" \
  "v_mfma_f32_16x16x32_bf16 v[28:31], v[116:119], v[92:95], v[28:31]\n\t" \
  "ds_read_b128 v[168:171], v183 offset:34816\n\t" \
  "v_mfma_f32_16x16x32_bf16 v[32:35], v[120:123], v[88:91], v[32:35]\n\t" \
  "ds_read_b128 v[172:175], v183 offset:35840\n\t" \
  "v_mfma_f32_16x16x32_bf16 v[36:39], v[120:123], v[92:95], v[36:39]\n\t" \
  "v_mfma_f32_16x16x32_bf16 v[40:43], v[124:127], v[88:91], v[40:43]\n\t" \
  "v_mfma_f32_16x16x32_bf16 v[44:47], v[124:127], v[92:95], v[44:47]\n\t" \
  "v_mfma_f32_16x16x32_bf16 v[48:51], v[128:131], v[88:91], v[48:51]\n\t" \
  "v_mfma_f32_16x16x32_bf16 v[52:55], v[128:131], v[92:95], v[52:55]\n\t" \
  "v_mfma_f32_16x16x32_bf16 v[56:59], v[132:135], v[88:91], v[56:59]\n\t" \
  "v_mfma_f32_16x16x32_bf16 v[60:63], v[132:135], v[92:95], v[60:63]\n\t" \
  "v_mfma_f32_16x16x32_bf16 v[64:67], v[136:139], v[88:91], v[64:67]\n\t" \
  "v_mfma_f32_16x16x32_bf16 v[68:71], v[136:139], v[92:95], v[68:71]\n\t" \
  "s_waitcnt lgkmcnt(0)\n\t" \
  "v_mfma_f32_16x16x32_bf16 v[0:3], v[140:143], v[96:99], v[0:3]\n\t" \
  "v_mfma_f32_16x16x32_bf16 v[4:7], v[140:143], v[100:103], v[4:7]\n\t" \
  "v_mfma_f32_16x16x32_bf16 v[8:11], v[144:147], v[96:99], v[8:11]\n\t" \
  "v_mfma_f32_16x16x32_bf16 v[12:15], v[144:147], v[100:103], v[12:15]\n\t" \
  "v_mfma_f32_16x16x32_bf16 v[16:19], v[148:151], v[96:99], v[16:19]\n\t" \
  "v_mfma_f32_16x16x32_bf16 v[20:23], v[148:151], v[100:103], v[20:23]\n\t" \
  "v_mfma_f32_16x16x32_bf16 v[24:27], v[152:155], v[96:99], v[24:27]\n\t" \
  "v_mfma_f32_16x16x32_bf16 v[28:31], v[152:155], v[100:103], v[28:31]\n\t" \
  "v_mfma_f32_16x16x32_bf16 v[32:35], v[156:159], v[96:99], v[32:35]\n\t" \
  "v_mfma_f32_16x16x32_bf16 v[36:39], v[156:159], v[100:103], v[36:39]\n\t" \
  "v_mfma_f32_16x16x32_bf16 v[40:43], v[160:163], v[96:99], v[40:43]\n\t" \
  "v_mfma_f32_16x16x32_bf16 v[44:47], v[160:163], v[100:103], v[44:47]\n\t" \
  "v_mfma_f32_16x16x32_bf16 v[48:51], v[164:167], v[96:99], v[48:51]\n\t" \
  "v_mfma_f32_16x16x32_bf16 v[52:55], v[164:167], v[100:103], v[52:55]\n\t" \
  "v_mfma_f32_16x16x32_bf16 v[56:59], v[168:171], v[96:99], v[56:59]\n\t" \
  "v_mfma_f32_16x16x32_bf16 v[60:63], v[168:171], v[100:103], v[60:63]\n\t" \
  "v_mfma_f32_16x16x32_bf16 v[64:67], v[172:175], v[96:99], v[64:67]\n\t" \
  "v_mfma_f32_16x16x32_bf16 v[68:71], v[172:175], v[100:103], v[68:71]\n\t" \
  "s_nop 15\n\t"
#define B2_CLOB "v72","v73","v74","v75","v76","v77","v78","v79","v80","v81","v82","v83","v84","v85","v86","v87","v88","v89","v90","v91","v92","v93","v94","v95","v96","v97","v98","v99","v100","v101","v102","v103","v104","v105","v106","v107","v108","v109","v110","v111","v112","v113","v114","v115","v116","v117","v118","v119","v120","v121","v122","v123","v124","v125","v126","v127","v128","v129","v130","v131","v132","v133","v134","v135","v136","v137","v138","v139","v140","v141","v142","v143","v144","v145","v146","v147","v148","v149","v150","v151","v152","v153","v154","v155","v156","v157","v158","v159","v160","v161","v162","v163","v164","v165","v166","v167","v168","v169","v170","v171","v172","v173","v174","v175"
#define B1_LOOP_ASM \
  "s_add_u32 m0, %[mc], 0\n\t" \
  "s_nop 0\n\t" \
  "buffer_load_dwordx4 v176, %[rc], %[sc] offen offset:0 lds\n\t" \
  "buffer_load_dwordx4 v177, %[rc], %[sc] offen offset:1024 lds\n\t" \
  "buffer_load_dwordx4 v178, %[rc], %[sc] offen offset:2048 lds\n\t" \
  "buffer_load_dwordx4 v179, %[rc], %[sc] offen offset:3072 lds\n\t" \
  "s_add_u32 m0, %[mb], 0\n\t" \
  "s_nop 0\n\t" \
  "buffer_load_dwordx4 v180, %[rb], %[sb] offen offset:0 lds\n\t" \
  "buffer_load_dwordx4 v181, %[rb], %[sb] offen offset:1024 lds\n\t" \
  "buffer_load_dwordx4 v182, %[rb], %[sb] offen offset:2048 lds\n\t" \
  "s_add_u32 %[sc], %[sc], 128\n\t" \
  "s_add_u32 %[sb], %[sb], 128\n\t" \
  "s_add_u32 m0, %[mc], 28672\n\t" \
  "s_nop 0\n\t" \
  "buffer_load_dwordx4 v176, %[rc], %[sc] offen offset:0 lds\n\t" \
  "buffer_load_dwordx4 v177, %[rc], %[sc] offen offset:1024 lds\n\t" \
  "buffer_load_dwordx4 v178, %[rc], %[sc] offen offset:2048 lds\n\t" \
  "buffer_load_dwordx4 v179, %[rc], %[sc] offen offset:3072 lds\n\t" \
  "s_add_u32 m0, %[mb], 28672\n\t" \
  "s_nop 0\n\t" \
  "buffer_load_dwordx4 v180, %[rb], %[sb] offen offset:0 lds\n\t" \
  "buffer_load_dwordx4 v181, %[rb], %[sb] offen offset:1024 lds\n\t" \
  "buffer_load_dwordx4 v182, %[rb], %[sb] offen offset:2048 lds\n\t" \
  "s_add_u32 %[sc], %[sc], 128\n\t" \
  "s_add_u32 %[sb], %[sb], 128\n\t" \
  "s_waitcnt vmcnt(7)\n\t" \
  "s_barrier\n\t" \
  "ds_read_b128 v[48:51], v183 offset:0\n\t" \
  "ds_read_b128 v[52:55], v183 offset:2048\n\t" \
  "ds_read_b128 v[64:67], v185 offset:0\n\t" \
  "ds_read_b128 v[68:71], v185 offset:2048\n\t" \
  "ds_read_b128 v[72:75], v185 offset:4096\n\t" \
  "ds_read_b128 v[76:79], v185 offset:6144\n\t" \
  "ds_read_b128 v[80:83], v185 offset:8192\n\t" \
  "ds_read_b128 v[84:87], v185 offset:10240\n\t" \
  "ds_read_b128 v[56:59], v184 offset:0\n\t" \
  "ds_read_b128 v[60:63], v184 offset:2048\n\t" \
  "ds_read_b128 v[88:91], v186 offset:0\n\t" \
  "ds_read_b128 v[92:95], v186 offset:2048\n\t" \
  "ds_read_b128 v[96:99], v186 offset:4096\n\t" \
  "ds_read_b128 v[100:103], v186 offset:6144\n\t" \
  "ds_read_b128 v[104:107], v186 offset:8192\n\t" \
  "ds_read_b128 v[108:111], v186 offset:10240\n\t" \
  "s_waitcnt lgkmcnt(0)\n\t" \
  "s_barrier\n\t" \
  "s_add_u32 m0, %[mc], 0\n\t" \
  "s_nop 0\n\t" \
  "buffer_load_dwordx4 v176, %[rc], %[sc] offen offset:0 lds\n\t" \
  "buffer_load_dwordx4 v177, %[rc], %[sc] offen offset:1024 lds\n\t" \
  "buffer_load_dwordx4 v178, %[rc], %[sc] offen offset:2048 lds\n\t" \
  "buffer_load_dwordx4 v179, %[rc], %[sc] offen offset:3072 lds\n\t" \
  "s_add_u32 m0, %[mb], 0\n\t" \
  "s_nop 0\n\t" \
  "buffer_load_dwordx4 v180, %[rb], %[sb] offen offset:0 lds\n\t" \
  "buffer_load_dwordx4 v181, %[rb], %[sb] offen offset:1024 lds\n\t" \
  "buffer_load_dwordx4 v182, %[rb], %[sb] offen offset:2048 lds\n\t" \
  "s_add_u32 %[sc], %[sc], 128\n\t" \
  "s_add_u32 %[sb], %[sb], 128\n\t" \
  "s_movk_i32 %[cnt], 3\n\t" \
  ".p2align 6\n\t" \
  "1:\n\t" \
  "s_waitcnt vmcnt(7)\n\t" \
  "s_barrier\n\t" \
  "v_mfma_f32_16x16x32_bf16 v[0:3], v[48:51], v[64:67], v[0:3]\n\t" \
  "ds_read_b128 v[112:115], v183 offset:28672\n\t" \
  "ds_read_b128 v[116:119], v183 offset:30720\n\t" \
  "v_mfma_f32_16x16x32_bf16 v[4:7], v[52:55], v[64:67], v[4:7]\n\t" \
  "ds_read_b128 v[128:131], v185 offset:28672\n\t" \
  "ds_read_b128 v[132:135], v185 offset:30720\n\t" \
  "v_mfma_f32_16x16x32_bf16 v[8:11], v[48:51], v[68:71], v[8:11]\n\t" \
  "ds_read_b128 v[136:139], v185 offset:32768\n\t" \
  "ds_read_b128 v[140:143], v185 offset:34816\n\t" \
  "v_mfma_f32_16x16x32_bf16 v[12:15], v[52:55], v[68:71], v[12:15]\n\t" \
  "ds_read_b128 v[144:147], v185 offset:36864\n\t" \
  "ds_read_b128 v[148:151], v185 offset:38912\n\t" \
  "v_mfma_f32_16x16x32_bf16 v[16:19], v[48:51], v[72:75], v[16:19]\n\t" \
  "ds_read_b128 v[120:123], v184 offset:28672\n\t" \
  "v_mfma_f32_16x16x32_bf16 v[20:23], v[52:55], v[72:75], v[20:23]\n\t" \
  "ds_read_b128 v[124:127], v184 offset:30720\n\t" \
  "v_mfma_f32_16x16x32_bf16 v[24:27], v[48:51], v[76:79], v[24:27]\n\t" \
  "ds_read_b128 v[152:155], v186 offset:28672\n\t" \
  "v_mfma_f32_16x16x32_bf16 v[28:31], v[52:55], v[76:79], v[28:31]\n\t" \
  "ds_read_b128 v[156:159], v186 offset:30720\n\t" \
  "v_mfma_f32_16x16x32_bf16 v[32:35], v[48:51], v[80:83], v[32:35]\n\t" \
  "ds_read_b128 v[160:163], v186 offset:32768\n\t" \
  "v_mfma_f32_16x16x32_bf16 v[36:39], v[52:55], v[80:83], v[36:39]\n\t" \
  "ds_read_b128 v[164:167], v186 offset:34816\n\t" \
  "v_mfma_f32_16x16x32_bf16 v[40:43], v[48:51], v[84:87], v[40:43]\n\t" \
  "ds_read_b128 v[168:171], v186 offset:36864\n\t" \
  "v_mfma_f32_16x16x32_bf16 v[44:47], v[52:55], v[84:87], v[44:47]\n\t" \
  "ds_read_b128 v[172:175], v186 offset:38912\n\t" \
  "s_waitcnt lgkmcnt(0)\n\t" \
  "s_barrier\n\t" \
  "v_mfma_f32_16x16x32_bf16 v[0:3], v[56:59], v[88:91], v[0:3]\n\t" \
  "s_add_u32 m0, %[mc], 28672\n\t" \
  "v_mfma_f32_16x16x32_bf16 v[4:7], v[60:63], v[88:91], v[4:7]\n\t" \
  "buffer_load_dwordx4 v176, %[rc], %[sc] offen offset:0 lds\n\t" \
  "v_mfma_f32_16x16x32_bf16 v[8:11], v[56:59], v[92:95], v[8:11]\n\t" \
  "buffer_load_dwordx4 v177, %[rc], %[sc] offen offset:1024 lds\n\t" \
  "v_mfma_f32_16x16x32_bf16 v[12:15], v[60:63], v[92:95], v[12:15]\n\t" \
  "buffer_load_dwordx4 v178, %[rc], %[sc] offen offset:2048 lds\n\t" \
  "v_mfma_f32_16x16x32_bf16 v[16:19], v[56:59], v[96:99], v[16:19]\n\t" \
  "buffer_load_dwordx4 v179, %[rc], %[sc] offen offset:3072 lds\n\t" \
  "s_add_u32 m0, %[mb], 28672\n\t" \
  "v_mfma_f32_16x16x32_bf16 v[20:23], v[60:63], v[96:99], v[20:23]\n\t" \
  "buffer_load_dwordx4 v180, %[rb], %[sb] offen offset:0 lds\n\t" \
  "v_mfma_f32_16x16x32_bf16 v[24:27], v[56:59], v[100:103], v[24:27]\n\t" \
  "buffer_load_dwordx4 v181, %[rb], %[sb] offen offset:1024 lds\n\t" \
  "v_mfma_f32_16x16x32_bf16 v[28:31], v[60:63], v[100:103], v[28:31]\n\t" \
  "buffer_load_dwordx4 v182, %[rb], %[sb] offen offset:2048 lds\n\t" \
  "v_mfma_f32_16x16x32_bf16 v[32:35], v[56:59], v[104:107], v[32:35]\n\t" \
  "s_add_u32 %[sc], %[sc], 128\n\t" \
  "s_add_u32 %[sb], %[sb], 128\n\t" \
  "v_mfma_f32_16x16x32_bf16 v[36:39], v[60:63], v[104:107], v[36:39]\n\t" \
  "v_mfma_f32_16x16x32_bf16 v[40:43], v[56:59], v[108:111], v[40:43]\n\t" \
  "v_mfma_f32_16x16x32_bf16 v[44:47], v[60:63], v[108:111], v[44:47]\n\t" \
  "s_waitcnt vmcnt(7)\n\t" \
  "s_barrier\n\t" \
  "v_mfma_f32_16x16x32_bf16 v[0:3], v[112:115], v[128:131], v[0:3]\n\t" \
  "ds_read_b128 v[48:51], v183 offset:0\n\t" \
  "ds_read_b128 v[52:55], v183 offset:2048\n\t" \
  "v_mfma_f32_16x16x32_bf16 v[4:7], v[116:119], v[128:131], v[4:7]\n\t" \
  "ds_read_b128 v[64:67], v185 offset:0\n\t" \
  "ds_read_b128 v[68:71], v185 offset:2048\n\t" \
  "v_mfma_f32_16x16x32_bf16 v[8:11], v[112:115], v[132:135], v[8:11]\n\t" \
  "ds_read_b128 v[72:75], v185 offset:4096\n\t" \
  "ds_read_b128 v[76:79], v185 offset:6144\n\t" \
  "v_mfma_f32_16x16x32_bf16 v[12:15], v[116:119], v[132:135], v[12:15]\n\t" \
  "ds_read_b128 v[80:83], v185 offset:8192\n\t" \
  "ds_read_b128 v[84:87], v185 offset:10240\n\t" \
  "v_mfma_f32_16x16x32_bf16 v[16:19], v[112:115], v[136:139], v[16:19]\n\t" \
  "ds_read_b128 v[56:59], v184 offset:0\n\t" \
  "v_mfma_f32_16x16x32_bf16 v[20:23], v[116:119], v[136:139], v[20:23]\n\t" \
  "ds_read_b128 v[60:63], v184 offset:2048\n\t" \
  "v_mfma_f32_16x16x32_bf16 v[24:27], v[112:115], v[140:143], v[24:27]\n\t" \
  "ds_read_b128 v[88:91], v186 offset:0\n\t" \
  "v_mfma_f32_16x16x32_bf16 v[28:31], v[116:119], v[140:143], v[28:31]\n\t" \
  "ds_read_b128 v[92:95], v186 offset:2048\n\t" \
  "v_mfma_f32_16x16x32_bf16 v[32:35], v[112:115], v[144:147], v[32:35]\n\t" \
  "ds_read_b128 v[96:99], v186 offset:4096\n\t" \
  "v_mfma_f32_16x16x32_bf16 v[36:39], v[116:119], v[144:147], v[36:39]\n\t" \
  "ds_read_b128 v[100:103], v186 offset:6144\n\t" \
  "v_mfma_f32_16x16x32_bf16 v[40:43], v[112:115], v[148:151], v[40:43]\n\t" \
  "ds_read_b128 v[104:107], v186 offset:8192\n\t" \
  "v_mfma_f32_16x16x32_bf16 v[44:47], v[116:119], v[148:151], v[44:47]\n\t" \
  "ds_read_b128 v[108:111], v186 offset:10240\n\t" \
  "s_waitcnt lgkmcnt(0)\n\t" \
  "s_barrier\n\t" \
  "v_mfma_f32_16x16x32_bf16 v[0:3], v[120:123], v[152:155], v[0:3]\n\t" \
  "s_add_u32 m0, %[mc], 0\n\t" \
  "v_mfma_f32_16x16x32_bf16 v[4:7], v[124:127], v[152:155], v[4:7]\n\t" \
  "buffer_load_dwordx4 v176, %[rc], %[sc] offen offset:0 lds\n\t" \
  "v_mfma_f32_16x16x32_bf16 v[8:11], v[120:123], v[156:159], v[8:11]\n\t" \
  "buffer_load_dwordx4 v177, %[rc], %[sc] offen offset:1024 lds\n\t" \
  "v_mfma_f32_16x16x32_bf16 v[12:15], v[124:127], v[156:159], v[12:15]\n\t" \
  "buffer_load_dwordx4 v178, %[rc], %[sc] offen offset:2048 lds\n\t" \
  "v_mfma_f32_16x16x32_bf16 v[16:19], v[120:123], v[160:163], v[16:19]\n\t" \
  "buffer_load_dwordx4 v179, %[rc], %[sc] offen offset:3072 lds\n\t" \
  "s_add_u32 m0, %[mb], 0\n\t" \
  "v_mfma_f32_16x16x32_bf16 v[20:23], v[124:127], v[160:163], v[20:23]\n\t" \
  "buffer_load_dwordx4 v180, %[rb], %[sb] offen offset:0 lds\n\t" \
  "v_mfma_f32_16x16x32_bf16 v[24:27], v[120:123], v[164:167], v[24:27]\n\t" \
  "buffer_load_dwordx4 v181, %[rb], %[sb] offen offset:1024 lds\n\t" \
  "v_mfma_f32_16x16x32_bf16 v[28:31], v[124:127], v[164:167], v[28:31]\n\t" \
  "buffer_load_dwordx4 v182, %[rb], %[sb] offen offset:2048 lds\n\t" \
  "v_mfma_f32_16x16x32_bf16 v[32:35], v[120:123], v[168:171], v[32:35]\n\t" \
  "s_add_u32 %[sc], %[sc], 128\n\t" \
  "s_add_u32 %[sb], %[sb], 128\n\t" \
  "v_mfma_f32_16x16x32_bf16 v[36:39], v[124:127], v[168:171], v[36:39]\n\t" \
  "v_mfma_f32_16x16x32_bf16 v[40:43], v[120:123], v[172:175], v[40:43]\n\t" \
  "v_mfma_f32_16x16x32_bf16 v[44:47], v[124:127], v[172:175], v[44:47]\n\t" \
  "s_addk_i32 %[cnt], -1\n\t" \
  "s_cmp_lg_u32 %[cnt], 0\n\t" \
  "s_cbranch_scc1 1b\n\t" \
  "s_waitcnt vmcnt(7)\n\t" \
  "s_barrier\n\t" \
  "v_mfma_f32_16x16x32_bf16 v[0:3], v[48:51], v[64:67], v[0:3]\n\t" \
  "ds_read_b128 v[112:115], v183 offset:28672\n\t" \
  "ds_read_b128 v[116:119], v183 offset:30720\n\t" \
  "v_mfma_f32_16x16x32_bf16 v[4:7], v[52:55], v[64:67], v[4:7]\n\t" \
  "ds_read_b128 v[128:131], v185 offset:28672\n\t" \
  "ds_read_b128 v[132:135], v185 offset:30720\n\t" \
  "v_mfma_f32_16x16x32_bf16 v[8:11], v[48:51], v[68:71], v[8:11]\n\t" \
  "ds_read_b128 v[136:139], v185 offset:32768\n\t" \
  "ds_read_b128 v[140:143], v185 offset:34816\n\t" \
  "v_mfma_f32_16x16x32_bf16 v[12:15], v[52:55], v[68:71], v[12:15]\n\t" \
  "ds_read_b128 v[144:147], v185 offset:36864\n\t" \
  "ds_read_b128 v[148:151], v185 offset:38912\n\t" \
  "v_mfma_f32_16x16x32_bf16 v[16:19], v[48:51], v[72:75], v[16:19]\n\t" \
  "ds_read_b128 v[120:123], v184 offset:28672\n\t" \
  "v_mfma_f32_16x16x32_bf16 v[20:23], v[52:55], v[72:75], v[20:23]\n\t" \
  "ds_read_b128 v[124:127], v184 offset:30720\n\t" \
  "v_mfma_f32_16x16x32_bf16 v[24:27], v[48:51], v[76:79], v[24:27]\n\t" \
  "ds_read_b128 v[152:155], v186 offset:28672\n\t" \
  "v_mfma_f32_16x16x32_bf16 v[28:31], v[52:55], v[76:79], v[28:31]\n\t" \
  "ds_read_b128 v[156:159], v186 offset:30720\n\t" \
  "v_mfma_f32_16x16x32_bf16 v[32:35], v[48:51], v[80:83], v[32:35]\n\t" \
  "ds_read_b128 v[160:163], v186 offset:32768\n\t" \
  "v_mfma_f32_16x16x32_bf16 v[36:39], v[52:55], v[80:83], v[36:39]\n\t" \
  "ds_read_b128 v[164:167], v186 offset:34816\n\t" \
  "v_mfma_f32_16x16x32_bf16 v[40:43], v[48:51], v[84:87], v[40:43]\n\t" \
  "ds_read_b128 v[168:171], v186 offset:36864\n\t" \
  "v_mfma_f32_16x16x32_bf16 v[44:47], v[52:55], v[84:87], v[44:47]\n\t" \
  "ds_read_b128 v[172:175], v186 offset:38912\n\t" \
  "s_waitcnt lgkmcnt(0)\n\t" \
  "v_mfma_f32_16x16x32_bf16 v[0:3], v[56:59], v[88:91], v[0:3]\n\t" \
  "v_mfma_f32_16x16x32_bf16 v[4:7], v[60:63], v[88:91], v[4:7]\n\t" \
  "v_mfma_f32_16x16x32_bf16 v[8:11], v[56:59], v[92:95], v[8:11]\n\t" \
  "v_mfma_f32_16x16x32_bf16 v[12:15], v[60:63], v[92:95], v[12:15]\n\t" \
  "v_mfma_f32_16x16x32_bf16 v[16:19], v[56:59], v[96:99], v[16:19]\n\t" \
  "v_mfma_f32_16x16x32_bf16 v[20:23], v[60:63], v[96:99], v[20:23]\n\t" \
  "v_mfma_f32_16x16x32_bf16 v[24:27], v[56:59], v[100:103], v[24:27]\n\t" \
  "v_mfma_f32_16x16x32_bf16 v[28:31], v[60:63], v[100:103], v[28:31]\n\t" \
  "v_mfma_f32_16x16x32_bf16 v[32:35], v[56:59], v[104:107], v[32:35]\n\t" \
  "v_mfma_f32_16x16x32_bf16 v[36:39], v[60:63], v[104:107], v[36:39]\n\t" \
  "v_mfma_f32_16x16x32_bf16 v[40:43], v[56:59], v[108:111], v[40:43]\n\t" \
  "v_mfma_f32_16x16x32_bf16 v[44:47], v[60:63], v[108:111], v[44:47]\n\t" \
  "s_waitcnt vmcnt(0)\n\t" \
  "s_barrier\n\t" \
  "v_mfma_f32_16x16x32_bf16 v[0:3], v[112:115], v[128:131], v[0:3]\n\t" \
  "ds_read_b128 v[48:51], v183 offset:0\n\t" \
  "ds_read_b128 v[52:55], v183 offset:2048\n\t" \
  "v_mfma_f32_16x16x32_bf16 v[4:7], v[116:119], v[128:131], v[4:7]\n\t" \
  "ds_read_b128 v[64:67], v185 offset:0\n\t" \
  "ds_read_b128 v[68:71], v185 offset:2048\n\t" \
  "v_mfma_f32_16x16x32_bf16 v[8:11], v[112:115], v[132:135], v[8:11]\n\t" \
  "ds_read_b128 v[72:75], v185 offset:4096\n\t" \
  "ds_read_b128 v[76:79], v185 offset:6144\n\t" \
  "v_mfma_f32_16x16x32_bf16 v[12:15], v[116:119], v[132:135], v[12:15]\n\t" \
  "ds_read_b128 v[80:83], v185 offset:8192\n\t" \
  "ds_read_b128 v[84:87], v185 offset:10240\n\t" \
  "v_mfma_f32_16x16x32_bf16 v[16:19], v[112:115], v[136:139], v[16:19]\n\t" \
  "ds_read_b128 v[56:59], v184 offset:0\n\t" \
  "v_mfma_f32_16x16x32_bf16 v[20:23], v[116:119], v[136:139], v[20:23]\n\t" \
  "ds_read_b128 v[60:63], v184 offset:2048\n\t" \
  "v_mfma_f32_16x16x32_bf16 v[24:27], v[112:115], v[140:143], v[24:27]\n\t" \
  "ds_read_b128 v[88:91], v186 offset:0\n\t" \
  "v_mfma_f32_16x16x32_bf16 v[28:31], v[116:119], v[140:143], v[28:31]\n\t" \
  "ds_read_b128 v[92:95], v186 offset:2048\n\t" \
  "v_mfma_f32_16x16x32_bf16 v[32:35], v[112:115], v[144:147], v[32:35]\n\t" \
  "ds_read_b128 v[96:99], v186 offset:4096\n\t" \
  "v_mfma_f32_16x16x32_bf16 v[36:39], v[116:119], v[144:147], v[36:39]\n\t" \
  "ds_read_b128 v[100:103], v186 offset:6144\n\t" \
  "v_mfma_f32_16x16x32_bf16 v[40:43], v[112:115], v[148:151], v[40:43]\n\t" \
  "ds_read_b128 v[104:107], v186 offset:8192\n\t" \
  "v_mfma_f32_16x16x32_bf16 v[44:47], v[116:119], v[148:151], v[44:47]\n\t" \
  "ds_read_b128 v[108:111], v186 offset:10240\n\t" \
  "s_waitcnt lgkmcnt(0)\n\t" \
  "v_mfma_f32_16x16x32_bf16 v[0:3], v[120:123], v[152:155], v[0:3]\n\t" \
  "v_mfma_f32_16x16x32_bf16 v[4:7], v[124:127], v[152:155], v[4:7]\n\t" \
  "v_mfma_f32_16x16x32_bf16 v[8:11], v[120:123], v[156:159], v[8:11]\n\t" \
  "v_mfma_f32_16x16x32_bf16 v[12:15], v[124:127], v[156:159], v[12:15]\n\t" \
  "v_mfma_f32_16x16x32_bf16 v[16:19], v[120:123], v[160:163], v[16:19]\n\t" \
  "v_mfma_f32_16x16x32_bf16 v[20:23], v[124:127], v[160:163], v[20:23]\n\t" \
  "v_mfma_f32_16x16x32_bf16 v[24:27], v[120:123], v[164:167], v[24:27]\n\t" \
  "v_mfma_f32_16x16x32_bf16 v[28:31], v[124:127], v[164:167], v[28:31]\n\t" \
  "v_mfma_f32_16x16x32_bf16 v[32:35], v[120:123], v[168:171], v[32:35]\n\t" \
  "v_mfma_f32_16x16x32_bf16 v[36:39], v[124:127], v[168:171], v[36:39]\n\t" \
  "v_mfma_f32_16x16x32_bf16 v[40:43], v[120:123], v[172:175], v[40:43]\n\t" \
  "v_mfma_f32_16x16x32_bf16 v[44:47], v[124:127], v[172:175], v[44:47]\n\t" \
  "v_mfma_f32_16x16x32_bf16 v[0:3], v[48:51], v[64:67], v[0:3]\n\t" \
  "v_mfma_f32_16x16x32_bf16 v[4:7], v[52:55], v[64:67], v[4:7]\n\t" \
  "v_mfma_f32_16x16x32_bf16 v[8:11], v[48:51], v[68:71], v[8:11]\n\t" \
  "v_mfma_f32_16x16x32_bf16 v[12:15], v[52:55], v[68:71], v[12:15]\n\t" \
  "v_mfma_f32_16x16x32_bf16 v[16:19], v[48:51], v[72:75], v[16:19]\n\t" \
  "v_mfma_f32_16x16x32_bf16 v[20:23], v[52:55], v[72:75], v[20:23]\n\t" \
  "v_mfma_f32_16x16x32_bf16 v[24:27], v[48:51], v[76:79], v[24:27]\n\t" \
  "v_mfma_f32_16x16x32_bf16 v[28:31], v[52:55], v[76:79], v[28:31]\n\t" \
  "v_mfma_f32_16x16x32_bf16 v[32:35], v[48:51], v[80:83], v[32:35]\n\t" \
  "v_mfma_f32_16x16x32_bf16 v[36:39], v[52:55], v[80:83], v[36:39]\n\t" \
  "v_mfma_f32_16x16x32_bf16 v[40:43], v[48:51], v[84:87], v[40:43]\n\t" \
  "v_mfma_f32_16x16x32_bf16 v[44:47], v[52:55], v[84:87], v[44:47]\n\t" \
  "s_waitcnt lgkmcnt(0)\n\t" \
  "v_mfma_f32_16x16x32_bf16 v[0:3], v[56:59], v[88:91], v[0:3]\n\t" \
  "v_mfma_f32_16x16x32_bf16 v[4:7], v[60:63], v[88:91], v[4:7]\n\t" \
  "v_mfma_f32_16x16x32_bf16 v[8:11], v[56:59], v[92:95], v[8:11]\n\t" \
  "v_mfma_f32_16x16x32_bf16 v[12:15], v[60:63], v[92:95], v[12:15]\n\t" \
  "v_mfma_f32_16x16x32_bf16 v[16:19], v[56:59], v[96:99], v[16:19]\n\t" \
  "v_mfma_f32_16x16x32_bf16 v[20:23], v[60:63], v[96:99], v[20:23]\n\t" \
  "v_mfma_f32_16x16x32_bf16 v[24:27], v[56:59], v[100:103], v[24:27]\n\t" \
  "v_mfma_f32_16x16x32_bf16 v[28:31], v[60:63], v[100:103], v[28:31]\n\t" \
  "v_mfma_f32_16x16x32_bf16 v[32:35], v[56:59], v[104:107], v[32:35]\n\t" \
  "v_mfma_f32_16x16x32_bf16 v[36:39], v[60:63], v[104:107], v[36:39]\n\t" \
  "v_mfma_f32_16x16x32_bf16 v[40:43], v[56:59], v[108:111], v[40:43]\n\t" \
  "v_mfma_f32_16x16x32_bf16 v[44:47], v[60:63], v[108:111], v[44:47]\n\t" \
  "s_nop 15\n\t"
#define B1_CLOB "v48","v49","v50","v51","v52","v53","v54","v55","v56","v57","v58","v59","v60","v61","v62","v63","v64","v65","v66","v67","v68","v69","v70","v71","v72","v73","v74","v75","v76","v77","v78","v79","v80","v81","v82","v83","v84","v85","v86","v87","v88","v89","v90","v91","v92","v93","v94","v95","v96","v97","v98","v99","v100","v101","v102","v103","v104","v105","v106","v107","v108","v109","v110","v111","v112","v113","v114","v115","v116","v117","v118","v119","v120","v121","v122","v123","v124","v125","v126","v127","v128","v129","v130","v131","v132","v133","v134","v135","v136","v137","v138","v139","v140","v141","v142","v143","v144","v145","v146","v147","v148","v149","v150","v151","v152","v153","v154","v155","v156","v157","v158","v159","v160","v161","v162","v163","v164","v165","v166","v167","v168","v169","v170","v171","v172","v173","v174","v175"
#define QP0_ASM \
  "s_add_u32 m0, %[mx], 0\n\t" \
  "s_nop 0\n\t" \
  "buffer_load_dwordx4 v128, %[rx], %[sx] offen lds\n\t" \
  "buffer_load_dwordx4 v129, %[rx], %[sx] offen offset:1024 lds\n\t" \
  "s_add_u32 m0, %[mx], 8192\n\t" \
  "s_nop 0\n\t" \
  "buffer_load_dwordx4 v130, %[rw], %[sw] offen lds\n\t" \
  "s_add_u32 %[sx], %[sx], 128\n\t" \
  "buffer_load_dwordx4 v131, %[rw], %[sw] offen offset:1024 lds\n\t" \
  "s_cmp_eq_u32 %[ex], 0\n\t" \
  "s_cbranch_scc1 19f\n\t" \
  "buffer_load_dwordx4 v132, %[rw], %[sw] offen offset:2048 lds\n\t" \
  "19:\n\t" \
  "s_add_u32 %[sw], %[sw], 73728\n\t" \
  "s_add_u32 m0, %[mx], 17408\n\t" \
  "s_nop 0\n\t" \
  "buffer_load_dwordx4 v128, %[rx], %[sx] offen lds\n\t" \
  "buffer_load_dwordx4 v129, %[rx], %[sx] offen offset:1024 lds\n\t" \
  "s_add_u32 m0, %[mx], 25600\n\t" \
  "s_nop 0\n\t" \
  "buffer_load_dwordx4 v130, %[rw], %[sw] offen lds\n\t" \
  "s_add_u32 %[sx], %[sx], 128\n\t" \
  "buffer_load_dwordx4 v131, %[rw], %[sw] offen offset:1024 lds\n\t" \
  "s_cmp_eq_u32 %[ex], 0\n\t" \
  "s_cbranch_scc1 20f\n\t" \
  "buffer_load_dwordx4 v132, %[rw], %[sw] offen offset:2048 lds\n\t" \
  "20:\n\t" \
  "s_add_u32 %[sw], %[sw], 73728\n\t" \
  "s_add_u32 m0, %[mx], 34816\n\t" \
  "s_nop 0\n\t" \
  "buffer_load_dwordx4 v128, %[rx], %[sx] offen lds\n\t" \
  "buffer_load_dwordx4 v129, %[rx], %[sx] offen offset:1024 lds\n\t" \
  "s_add_u32 m0, %[mx], 43008\n\t" \
  "s_nop 0\n\t" \
  "buffer_load_dwordx4 v130, %[rw], %[sw] offen lds\n\t" \
  "s_add_u32 %[sx], %[sx], 128\n\t" \
  "buffer_load_dwordx4 v131, %[rw], %[sw] offen offset:1024 lds\n\t" \
  "s_cmp_eq_u32 %[ex], 0\n\t" \
  "s_cbranch_scc1 21f\n\t" \
  "buffer_load_dwordx4 v132, %[rw], %[sw] offen offset:2048 lds\n\t" \
  "21:\n\t" \
  "s_add_u32 %[sw], %[sw], 73728\n\t" \
  "s_add_u32 m0, %[mx], 52224\n\t" \
  "s_nop 0\n\t" \
  "buffer_load_dwordx4 v128, %[rx], %[sx] offen lds\n\t" \
  "buffer_load_dwordx4 v129, %[rx], %[sx] offen offset:1024 lds\n\t" \
  "s_add_u32 m0, %[mx], 60416\n\t" \
  "s_nop 0\n\t" \
  "buffer_load_dwordx4 v130, %[rw], %[sw] offen lds\n\t" \
  "s_add_u32 %[sx], %[sx], 128\n\t" \
  "buffer_load_dwordx4 v131, %[rw], %[sw] offen offset:1024 lds\n\t" \
  "s_cmp_eq_u32 %[ex], 0\n\t" \
  "s_cbranch_scc1 22f\n\t" \
  "buffer_load_dwordx4 v132, %[rw], %[sw] offen offset:2048 lds\n\t" \
  "22:\n\t" \
  "s_add_u32 %[sw], %[sw], 73728\n\t"
#define QP_ASM \
  "s_waitcnt vmcnt(12)\n\t" \
  "s_barrier\n\t" \
  "ds_read_b128 v[32:35], v133 offset:0\n\t" \
  "ds_read_b64_tr_b16 v[48:49], v137 offset:0\n\t" \
  "ds_read_b64_tr_b16 v[50:51], v137 offset:144\n\t" \
  "ds_read_b64_tr_b16 v[52:53], v137 offset:32\n\t" \
  "ds_read_b64_tr_b16 v[54:55], v137 offset:176\n\t" \
  "ds_read_b128 v[36:39], v134 offset:0\n\t" \
  "ds_read_b64_tr_b16 v[56:57], v137 offset:2304\n\t" \
  "ds_read_b64_tr_b16 v[58:59], v137 offset:2448\n\t" \
  "ds_read_b64_tr_b16 v[60:61], v137 offset:2336\n\t" \
  "ds_read_b64_tr_b16 v[62:63], v137 offset:2480\n\t" \
  "ds_read_b128 v[40:43], v135 offset:0\n\t" \
  "ds_read_b64_tr_b16 v[64:65], v137 offset:4608\n\t" \
  "ds_read_b64_tr_b16 v[66:67], v137 offset:4752\n\t" \
  "ds_read_b64_tr_b16 v[68:69], v137 offset:4640\n\t" \
  "ds_read_b64_tr_b16 v[70:71], v137 offset:4784\n\t" \
  "ds_read_b128 v[44:47], v136 offset:0\n\t" \
  "ds_read_b64_tr_b16 v[72:73], v137 offset:6912\n\t" \
  "ds_read_b64_tr_b16 v[74:75], v137 offset:7056\n\t" \
  "ds_read_b64_tr_b16 v[76:77], v137 offset:6944\n\t" \
  "ds_read_b64_tr_b16 v[78:79], v137 offset:7088\n\t" \
  "s_waitcnt vmcnt(8) lgkmcnt(0)\n\t" \
  "s_barrier\n\t" \
  "v_mfma_f32_32x32x16_bf16 v[0:15], v[48:51], v[32:35], 0\n\t" \
  "ds_read_b128 v[80:83], v133 offset:17408\n\t" \
  "ds_read_b64_tr_b16 v[96:97], v137 offset:17408\n\t" \
  "ds_read_b64_tr_b16 v[98:99], v137 offset:17552\n\t" \
  "buffer_load_dwordx2 v[140:141], off, %[rf], 0 sc1\n\t" \
  "v_mfma_f32_32x32x16_bf16 v[16:31], v[52:55], v[32:35], 0\n\t" \
  "ds_read_b64_tr_b16 v[100:101], v137 offset:17440\n\t" \
  "ds_read_b64_tr_b16 v[102:103], v137 offset:17584\n\t" \
  "v_mfma_f32_32x32x16_bf16 v[0:15], v[56:59], v[36:39], v[0:15]\n\t" \
  "ds_read_b128 v[84:87], v134 offset:17408\n\t" \
  "ds_read_b64_tr_b16 v[104:105], v137 offset:19712\n\t" \
  "ds_read_b64_tr_b16 v[106:107], v137 offset:19856\n\t" \
  "s_add_u32 m0, %[mx], 0\n\t" \
  "v_mfma_f32_32x32x16_bf16 v[16:31], v[60:63], v[36:39], v[16:31]\n\t" \
  "ds_read_b64_tr_b16 v[108:109], v137 offset:19744\n\t" \
  "ds_read_b64_tr_b16 v[110:111], v137 offset:19888\n\t" \
  "buffer_load_dwordx4 v128, %[rx], %[sx] offen lds\n\t" \
  "v_mfma_f32_32x32x16_bf16 v[0:15], v[64:67], v[40:43], v[0:15]\n\t" \
  "ds_read_b128 v[88:91], v135 offset:17408\n\t" \
  "ds_read_b64_tr_b16 v[112:113], v137 offset:22016\n\t" \
  "ds_read_b64_tr_b16 v[114:115], v137 offset:22160\n\t" \
  "buffer_load_dwordx4 v129, %[rx], %[sx] offen offset:1024 lds\n\t" \
  "s_add_u32 m0, %[mx], 8192\n\t" \
  "v_mfma_f32_32x32x16_bf16 v[16:31], v[68:71], v[40:43], v[16:31]\n\t" \
  "ds_read_b64_tr_b16 v[116:117], v137 offset:22048\n\t" \
  "ds_read_b64_tr_b16 v[118:119], v137 offset:22192\n\t" \
  "buffer_load_dwordx4 v130, %[rw], %[sw] offen lds\n\t" \
  "s_add_u32 %[sx], %[sx], 128\n\t" \
  "v_mfma_f32_32x32x16_bf16 v[0:15], v[72:75], v[44:47], v[0:15]\n\t" \
  "ds_read_b128 v[92:95], v136 offset:17408\n\t" \
  "ds_read_b64_tr_b16 v[120:121], v137 offset:24320\n\t" \
  "ds_read_b64_tr_b16 v[122:123], v137 offset:24464\n\t" \
  "buffer_load_dwordx4 v131, %[rw], %[sw] offen offset:1024 lds\n\t" \
  "v_mfma_f32_32x32x16_bf16 v[16:31], v[76:79], v[44:47], v[16:31]\n\t" \
  "ds_read_b64_tr_b16 v[124:125], v137 offset:24352\n\t" \
  "ds_read_b64_tr_b16 v[126:127], v137 offset:24496\n\t" \
  "s_cmp_eq_u32 %[ex], 0\n\t" \
  "s_cbranch_scc1 23f\n\t" \
  "buffer_load_dwordx4 v132, %[rw], %[sw] offen offset:2048 lds\n\t" \
  "23:\n\t" \
  "s_add_u32 %[sw], %[sw], 73728\n\t" \
  "s_waitcnt vmcnt(8) lgkmcnt(0)\n\t" \
  "s_barrier\n\t" \
  "v_mfma_f32_32x32x16_bf16 v[0:15], v[96:99], v[80:83], v[0:15]\n\t" \
  "ds_read_b128 v[32:35], v133 offset:34816\n\t" \
  "ds_read_b64_tr_b16 v[48:49], v137 offset:34816\n\t" \
  "ds_read_b64_tr_b16 v[50:51], v137 offset:34960\n\t" \
  "v_mfma_f32_32x32x16_bf16 v[16:31], v[100:103], v[80:83], v[16:31]\n\t" \
  "ds_read_b64_tr_b16 v[52:53], v137 offset:34848\n\t" \
  "ds_read_b64_tr_b16 v[54:55], v137 offset:34992\n\t" \
  "v_mfma_f32_32x32x16_bf16 v[0:15], v[104:107], v[84:87], v[0:15]\n\t" \
  "ds_read_b128 v[36:39], v134 offset:34816\n\t" \
  "ds_read_b64_tr_b16 v[56:57], v137 offset:37120\n\t" \
  "ds_read_b64_tr_b16 v[58:59], v137 offset:37264\n\t" \
  "s_add_u32 m0, %[mx], 17408\n\t" \
  "v_mfma_f32_32x32x16_bf16 v[16:31], v[108:111], v[84:87], v[16:31]\n\t" \
  "ds_read_b64_tr_b16 v[60:61], v137 offset:37152\n\t" \
  "ds_read_b64_tr_b16 v[62:63], v137 offset:37296\n\t" \
  "buffer_load_dwordx4 v128, %[rx], %[sx] offen lds\n\t" \
  "v_mfma_f32_32x32x16_bf16 v[0:15], v[112:115], v[88:91], v[0:15]\n\t" \
  "ds_read_b128 v[40:43], v135 offset:34816\n\t" \
  "ds_read_b64_tr_b16 v[64:65], v137 offset:39424\n\t" \
  "ds_read_b64_tr_b16 v[66:67], v137 offset:39568\n\t" \
  "buffer_load_dwordx4 v129, %[rx], %[sx] offen offset:1024 lds\n\t" \
  "s_add_u32 m0, %[mx], 25600\n\t" \
  "v_mfma_f32_32x32x16_bf16 v[16:31], v[116:119], v[88:91], v[16:31]\n\t" \
  "ds_read_b64_tr_b16 v[68:69], v137 offset:39456\n\t" \
  "ds_read_b64_tr_b16 v[70:71], v137 offset:39600\n\t" \
  "buffer_load_dwordx4 v130, %[rw], %[sw] offen lds\n\t" \
  "s_add_u32 %[sx], %[sx], 128\n\t" \
  "v_mfma_f32_32x32x16_bf16 v[0:15], v[120:123], v[92:95], v[0:15]\n\t" \
  "ds_read_b128 v[44:47], v136 offset:34816\n\t" \
  "ds_read_b64_tr_b16 v[72:73], v137 offset:41728\n\t" \
  "ds_read_b64_tr_b16 v[74:75], v137 offset:41872\n\t" \
  "buffer_load_dwordx4 v131, %[rw], %[sw] offen offset:1024 lds\n\t" \
  "v_mfma_f32_32x32x16_bf16 v[16:31], v[124:127], v[92:95], v[16:31]\n\t" \
  "ds_read_b64_tr_b16 v[76:77], v137 offset:41760\n\t" \
  "ds_read_b64_tr_b16 v[78:79], v137 offset:41904\n\t" \
  "s_cmp_eq_u32 %[ex], 0\n\t" \
  "s_cbranch_scc1 24f\n\t" \
  "buffer_load_dwordx4 v132, %[rw], %[sw] offen offset:2048 lds\n\t" \
  "24:\n\t" \
  "s_add_u32 %[sw], %[sw], 73728\n\t" \
  "s_waitcnt vmcnt(8) lgkmcnt(0)\n\t" \
  "s_barrier\n\t" \
  "v_mfma_f32_32x32x16_bf16 v[0:15], v[48:51], v[32:35], v[0:15]\n\t" \
  "ds_read_b128 v[80:83], v133 offset:52224\n\t" \
  "ds_read_b64_tr_b16 v[96:97], v137 offset:52224\n\t" \
  "ds_read_b64_tr_b16 v[98:99], v137 offset:52368\n\t" \
  "v_mfma_f32_32x32x16_bf16 v[16:31], v[52:55], v[32:35], v[16:31]\n\t" \
  "ds_read_b64_tr_b16 v[100:101], v137 offset:52256\n\t" \
  "ds_read_b64_tr_b16 v[102:103], v137 offset:52400\n\t" \
  "v_mfma_f32_32x32x16_bf16 v[0:15], v[56:59], v[36:39], v[0:15]\n\t" \
  "ds_read_b128 v[84:87], v134 offset:52224\n\t" \
  "ds_read_b64_tr_b16 v[104:105], v137 offset:54528\n\t" \
  "ds_read_b64_tr_b16 v[106:107], v137 offset:54672\n\t" \
  "s_add_u32 m0, %[mx], 34816\n\t" \
  "v_mfma_f32_32x32x16_bf16 v[16:31], v[60:63], v[36:39], v[16:31]\n\t" \
  "ds_read_b64_tr_b16 v[108:109], v137 offset:54560\n\t" \
  "ds_read_b64_tr_b16 v[110:111], v137 offset:54704\n\t" \
  "buffer_load_dwordx4 v128, %[rx], %[sx] offen lds\n\t" \
  "v_mfma_f32_32x32x16_bf16 v[0:15], v[64:67], v[40:43], v[0:15]\n\t" \
  "ds_read_b128 v[88:91], v135 offset:52224\n\t" \
  "ds_read_b64_tr_b16 v[112:113], v137 offset:56832\n\t" \
  "ds_read_b64_tr_b16 v[114:115], v137 offset:56976\n\t" \
  "buffer_load_dwordx4 v129, %[rx], %[sx] offen offset:1024 lds\n\t" \
  "s_add_u32 m0, %[mx], 43008\n\t" \
  "v_mfma_f32_32x32x16_bf16 v[16:31], v[68:71], v[40:43], v[16:31]\n\t" \
  "ds_read_b64_tr_b16 v[116:117], v137 offset:56864\n\t" \
  "ds_read_b64_tr_b16 v[118:119], v137 offset:57008\n\t" \
  "buffer_load_dwordx4 v130, %[rw], %[sw] offen lds\n\t" \
  "s_add_u32 %[sx], %[sx], 128\n\t" \
  "v_mfma_f32_32x32x16_bf16 v[0:15], v[72:75], v[44:47], v[0:15]\n\t" \
  "ds_read_b128 v[92:95], v136 offset:52224\n\t" \
  "ds_read_b64_tr_b16 v[120:121], v137 offset:59136\n\t" \
  "ds_read_b64_tr_b16 v[122:123], v137 offset:59280\n\t" \
  "buffer_load_dwordx4 v131, %[rw], %[sw] offen offset:1024 lds\n\t" \
  "v_mfma_f32_32x32x16_bf16 v[16:31], v[76:79], v[44:47], v[16:31]\n\t" \
  "ds_read_b64_tr_b16 v[124:125], v137 offset:59168\n\t" \
  "ds_read_b64_tr_b16 v[126:127], v137 offset:59312\n\t" \
  "s_cmp_eq_u32 %[ex], 0\n\t" \
  "s_cbranch_scc1 25f\n\t" \
  "buffer_load_dwordx4 v132, %[rw], %[sw] offen offset:2048 lds\n\t" \
  "25:\n\t" \
  "s_add_u32 %[sw], %[sw], 73728\n\t" \
  "s_waitcnt vmcnt(8) lgkmcnt(0)\n\t" \
  "s_barrier\n\t" \
  "v_mfma_f32_32x32x16_bf16 v[0:15], v[96:99], v[80:83], v[0:15]\n\t" \
  "ds_read_b128 v[32:35], v133 offset:0\n\t" \
  "ds_read_b64_tr_b16 v[48:49], v137 offset:0\n\t" \
  "ds_read_b64_tr_b16 v[50:51], v137 offset:144\n\t" \
  "v_mfma_f32_32x32x16_bf16 v[16:31], v[100:103], v[80:83], v[16:31]\n\t" \
  "ds_read_b64_tr_b16 v[52:53], v137 offset:32\n\t" \
  "ds_read_b64_tr_b16 v[54:55], v137 offset:176\n\t" \
  "v_mfma_f32_32x32x16_bf16 v[0:15], v[104:107], v[84:87], v[0:15]\n\t" \
  "ds_read_b128 v[36:39], v134 offset:0\n\t" \
  "ds_read_b64_tr_b16 v[56:57], v137 offset:2304\n\t" \
  "ds_read_b64_tr_b16 v[58:59], v137 offset:2448\n\t" \
  "s_add_u32 m0, %[mx], 52224\n\t" \
  "v_mfma_f32_32x32x16_bf16 v[16:31], v[108:111], v[84:87], v[16:31]\n\t" \
  "ds_read_b64_tr_b16 v[60:61], v137 offset:2336\n\t" \
  "ds_read_b64_tr_b16 v[62:63], v137 offset:2480\n\t" \
  "buffer_load_dwordx4 v128, %[rx], %[sx] offen lds\n\t" \
  "v_mfma_f32_32x32x16_bf16 v[0:15], v[112:115], v[88:91], v[0:15]\n\t" \
  "ds_read_b128 v[40:43], v135 offset:0\n\t" \
  "ds_read_b64_tr_b16 v[64:65], v137 offset:4608\n\t" \
  "ds_read_b64_tr_b16 v[66:67], v137 offset:4752\n\t" \
  "buffer_load_dwordx4 v129, %[rx], %[sx] offen offset:1024 lds\n\t" \
  "s_add_u32 m0, %[mx], 60416\n\t" \
  "v_mfma_f32_32x32x16_bf16 v[16:31], v[116:119], v[88:91], v[16:31]\n\t" \
  "ds_read_b64_tr_b16 v[68:69], v137 offset:4640\n\t" \
  "ds_read_b64_tr_b16 v[70:71], v137 offset:4784\n\t" \
  "buffer_load_dwordx4 v130, %[rw], %[sw] offen lds\n\t" \
  "s_add_u32 %[sx], %[sx], 128\n\t" \
  "v_mfma_f32_32x32x16_bf16 v[0:15], v[120:123], v[92:95], v[0:15]\n\t" \
  "ds_read_b128 v[44:47], v136 offset:0\n\t" \
  "ds_read_b64_tr_b16 v[72:73], v137 offset:6912\n\t" \
  "ds_read_b64_tr_b16 v[74:75], v137 offset:7056\n\t" \
  "buffer_load_dwordx4 v131, %[rw], %[sw] offen offset:1024 lds\n\t" \
  "v_mfma_f32_32x32x16_bf16 v[16:31], v[124:127], v[92:95], v[16:31]\n\t" \
  "ds_read_b64_tr_b16 v[76:77], v137 offset:6944\n\t" \
  "ds_read_b64_tr_b16 v[78:79], v137 offset:7088\n\t" \
  "s_cmp_eq_u32 %[ex], 0\n\t" \
  "s_cbranch_scc1 26f\n\t" \
  "buffer_load_dwordx4 v132, %[rw], %[sw] offen offset:2048 lds\n\t" \
  "26:\n\t" \
  "s_add_u32 %[sw], %[sw], 73728\n\t" \
  "s_waitcnt vmcnt(8) lgkmcnt(0)\n\t" \
  "s_barrier\n\t" \
  "v_mfma_f32_32x32x16_bf16 v[0:15], v[48:51], v[32:35], v[0:15]\n\t" \
  "ds_read_b128 v[80:83], v133 offset:17408\n\t" \
  "ds_read_b64_tr_b16 v[96:97], v137 offset:17408\n\t" \
  "ds_read_b64_tr_b16 v[98:99], v137 offset:17552\n\t" \
  "v_mfma_f32_32x32x16_bf16 v[16:31], v[52:55], v[32:35], v[16:31]\n\t" \
  "ds_read_b64_tr_b16 v[100:101], v137 offset:17440\n\t" \
  "ds_read_b64_tr_b16 v[102:103], v137 offset:17584\n\t" \
  "v_mfma_f32_32x32x16_bf16 v[0:15], v[56:59], v[36:39], v[0:15]\n\t" \
  "ds_read_b128 v[84:87], v134 offset:17408\n\t" \
  "ds_read_b64_tr_b16 v[104:105], v137 offset:19712\n\t" \
  "ds_read_b64_tr_b16 v[106:107], v137 offset:19856\n\t" \
  "s_add_u32 m0, %[mx], 0\n\t" \
  "v_mfma_f32_32x32x16_bf16 v[16:31], v[60:63], v[36:39], v[16:31]\n\t" \
  "ds_read_b64_tr_b16 v[108:109], v137 offset:19744\n\t" \
  "ds_read_b64_tr_b16 v[110:111], v137 offset:19888\n\t" \
  "buffer_load_dwordx4 v128, %[rx], %[sx] offen lds\n\t" \
  "v_mfma_f32_32x32x16_bf16 v[0:15], v[64:67], v[40:43], v[0:15]\n\t" \
  "ds_read_b128 v[88:91], v135 offset:17408\n\t" \
  "ds_read_b64_tr_b16 v[112:113], v137 offset:22016\n\t" \
  "ds_read_b64_tr_b16 v[114:115], v137 offset:22160\n\t" \
  "buffer_load_dwordx4 v129, %[rx], %[sx] offen offset:1024 lds\n\t" \
  "s_add_u32 m0, %[mx], 8192\n\t" \
  "v_mfma_f32_32x32x16_bf16 v[16:31], v[68:71], v[40:43], v[16:31]\n\t" \
  "ds_read_b64_tr_b16 v[116:117], v137 offset:22048\n\t" \
  "ds_read_b64_tr_b16 v[118:119], v137 offset:22192\n\t" \
  "buffer_load_dwordx4 v130, %[rw], %[sw] offen lds\n\t" \
  "s_add_u32 %[sx], %[sx], 128\n\t" \
  "v_mfma_f32_32x32x16_bf16 v[0:15], v[72:75], v[44:47], v[0:15]\n\t" \
  "ds_read_b128 v[92:95], v136 offset:17408\n\t" \
  "ds_read_b64_tr_b16 v[120:121], v137 offset:24320\n\t" \
  "ds_read_b64_tr_b16 v[122:123], v137 offset:24464\n\t" \
  "buffer_load_dwordx4 v131, %[rw], %[sw] offen offset:1024 lds\n\t" \
  "v_mfma_f32_32x32x16_bf16 v[16:31], v[76:79], v[44:47], v[16:31]\n\t" \
  "ds_read_b64_tr_b16 v[124:125], v137 offset:24352\n\t" \
  "ds_read_b64_tr_b16 v[126:127], v137 offset:24496\n\t" \
  "s_cmp_eq_u32 %[ex], 0\n\t" \
  "s_cbranch_scc1 27f\n\t" \
  "buffer_load_dwordx4 v132, %[rw], %[sw] offen offset:2048 lds\n\t" \
  "27:\n\t" \
  "s_add_u32 %[sw], %[sw], 73728\n\t" \
  "s_waitcnt vmcnt(8) lgkmcnt(0)\n\t" \
  "s_barrier\n\t" \
  "v_mfma_f32_32x32x16_bf16 v[0:15], v[96:99], v[80:83], v[0:15]\n\t" \
  "ds_read_b128 v[32:35], v133 offset:34816\n\t" \
  "ds_read_b64_tr_b16 v[48:49], v137 offset:34816\n\t" \
  "ds_read_b64_tr_b16 v[50:51], v137 offset:34960\n\t" \
  "buffer_load_dwordx4 v[142:145], v138, %[rf], 0 offen sc1\n\t" \
  "v_mfma_f32_32x32x16_bf16 v[16:31], v[100:103], v[80:83], v[16:31]\n\t" \
  "ds_read_b64_tr_b16 v[52:53], v137 offset:34848\n\t" \
  "ds_read_b64_tr_b16 v[54:55], v137 offset:34992\n\t" \
  "v_mfma_f32_32x32x16_bf16 v[0:15], v[104:107], v[84:87], v[0:15]\n\t" \
  "ds_read_b128 v[36:39], v134 offset:34816\n\t" \
  "ds_read_b64_tr_b16 v[56:57], v137 offset:37120\n\t" \
  "ds_read_b64_tr_b16 v[58:59], v137 offset:37264\n\t" \
  "v_mfma_f32_32x32x16_bf16 v[16:31], v[108:111], v[84:87], v[16:31]\n\t" \
  "ds_read_b64_tr_b16 v[60:61], v137 offset:37152\n\t" \
  "ds_read_b64_tr_b16 v[62:63], v137 offset:37296\n\t" \
  "v_mfma_f32_32x32x16_bf16 v[0:15], v[112:115], v[88:91], v[0:15]\n\t" \
  "ds_read_b128 v[40:43], v135 offset:34816\n\t" \
  "ds_read_b64_tr_b16 v[64:65], v137 offset:39424\n\t" \
  "ds_read_b64_tr_b16 v[66:67], v137 offset:39568\n\t" \
  "v_mfma_f32_32x32x16_bf16 v[16:31], v[116:119], v[88:91], v[16:31]\n\t" \
  "ds_read_b64_tr_b16 v[68:69], v137 offset:39456\n\t" \
  "ds_read_b64_tr_b16 v[70:71], v137 offset:39600\n\t" \
  "v_mfma_f32_32x32x16_bf16 v[0:15], v[120:123], v[92:95], v[0:15]\n\t" \
  "ds_read_b128 v[44:47], v136 offset:34816\n\t" \
  "ds_read_b64_tr_b16 v[72:73], v137 offset:41728\n\t" \
  "ds_read_b64_tr_b16 v[74:75], v137 offset:41872\n\t" \
  "v_mfma_f32_32x32x16_bf16 v[16:31], v[124:127], v[92:95], v[16:31]\n\t" \
  "ds_read_b64_tr_b16 v[76:77], v137 offset:41760\n\t" \
  "ds_read_b64_tr_b16 v[78:79], v137 offset:41904\n\t" \
  "s_waitcnt vmcnt(4) lgkmcnt(0)\n\t" \
  "s_barrier\n\t" \
  "v_mfma_f32_32x32x16_bf16 v[0:15], v[48:51], v[32:35], v[0:15]\n\t" \
  "ds_read_b128 v[80:83], v133 offset:52224\n\t" \
  "ds_read_b64_tr_b16 v[96:97], v137 offset:52224\n\t" \
  "ds_read_b64_tr_b16 v[98:99], v137 offset:52368\n\t" \
  "v_mfma_f32_32x32x16_bf16 v[16:31], v[52:55], v[32:35], v[16:31]\n\t" \
  "ds_read_b64_tr_b16 v[100:101], v137 offset:52256\n\t" \
  "ds_read_b64_tr_b16 v[102:103], v137 offset:52400\n\t" \
  "v_mfma_f32_32x32x16_bf16 v[0:15], v[56:59], v[36:39], v[0:15]\n\t" \
  "ds_read_b128 v[84:87], v134 offset:52224\n\t" \
  "ds_read_b64_tr_b16 v[104:105], v137 offset:54528\n\t" \
  "ds_read_b64_tr_b16 v[106:107], v137 offset:54672\n\t" \
  "v_mfma_f32_32x32x16_bf16 v[16:31], v[60:63], v[36:39], v[16:31]\n\t" \
  "ds_read_b64_tr_b16 v[108:109], v137 offset:54560\n\t" \
  "ds_read_b64_tr_b16 v[110:111], v137 offset:54704\n\t" \
  "v_mfma_f32_32x32x16_bf16 v[0:15], v[64:67], v[40:43], v[0:15]\n\t" \
  "ds_read_b128 v[88:91], v135 offset:52224\n\t" \
  "ds_read_b64_tr_b16 v[112:113], v137 offset:56832\n\t" \
  "ds_read_b64_tr_b16 v[114:115], v137 offset:56976\n\t" \
  "v_mfma_f32_32x32x16_bf16 v[16:31], v[68:71], v[40:43], v[16:31]\n\t" \
  "ds_read_b64_tr_b16 v[116:117], v137 offset:56864\n\t" \
  "ds_read_b64_tr_b16 v[118:119], v137 offset:57008\n\t" \
  "v_mfma_f32_32x32x16_bf16 v[0:15], v[72:75], v[44:47], v[0:15]\n\t" \
  "ds_read_b128 v[92:95], v136 offset:52224\n\t" \
  "ds_read_b64_tr_b16 v[120:121], v137 offset:59136\n\t" \
  "ds_read_b64_tr_b16 v[122:123], v137 offset:59280\n\t" \
  "v_mfma_f32_32x32x16_bf16 v[16:31], v[76:79], v[44:47], v[16:31]\n\t" \
  "ds_read_b64_tr_b16 v[124:125], v137 offset:59168\n\t" \
  "ds_read_b64_tr_b16 v[126:127], v137 offset:59312\n\t" \
  "s_waitcnt vmcnt(0) lgkmcnt(0)\n\t" \
  "v_add_co_u32 v140, vcc, 1, v140\n\t" \
  "s_movk_i32 %[cnt], 0x7fff\n\t" \
  "s_nop 1\n\t" \
  "v_addc_co_u32 v141, vcc, 0, v141, vcc\n\t" \
  "2:\n\t" \
  "v_cmp_ne_u64 vcc, v[140:141], v[142:143]\n\t" \
  "v_cmp_ne_u64 %[t2], v[140:141], v[144:145]\n\t" \
  "s_nop 4\n\t" \
  "s_or_b64 vcc, vcc, %[t2]\n\t" \
  "s_and_b64 vcc, vcc, %[fm]\n\t" \
  "s_cbranch_vccz 3f\n\t" \
  "s_sleep 4\n\t" \
  "buffer_load_dwordx4 v[142:145], v138, %[rf], 0 offen sc1\n\t" \
  "s_addk_i32 %[cnt], -1\n\t" \
  "s_cmp_lg_u32 %[cnt], 0\n\t" \
  "s_waitcnt vmcnt(0)\n\t" \
  "s_cbranch_scc1 2b\n\t" \
  "3:\n\t" \
  "s_barrier\n\t" \
  "v_mfma_f32_32x32x16_bf16 v[0:15], v[96:99], v[80:83], v[0:15]\n\t" \
  "s_add_u32 m0, %[mk], 40960\n\t" \
  "ds_read_b128 v[32:35], v133 offset:0\n\t" \
  "ds_read_b64_tr_b16 v[48:49], v137 offset:0\n\t" \
  "ds_read_b64_tr_b16 v[50:51], v137 offset:144\n\t" \
  "v_mfma_f32_32x32x16_bf16 v[16:31], v[100:103], v[80:83], v[16:31]\n\t" \
  "buffer_load_dwordx4 v139, %[rf], %[sk] offen offset:0 sc1 lds\n\t" \
  "ds_read_b64_tr_b16 v[52:53], v137 offset:32\n\t" \
  "ds_read_b64_tr_b16 v[54:55], v137 offset:176\n\t" \
  "v_mfma_f32_32x32x16_bf16 v[0:15], v[104:107], v[84:87], v[0:15]\n\t" \
  "buffer_load_dwordx4 v139, %[rf], %[sk] offen offset:1024 sc1 lds\n\t" \
  "ds_read_b128 v[36:39], v134 offset:0\n\t" \
  "ds_read_b64_tr_b16 v[56:57], v137 offset:2304\n\t" \
  "ds_read_b64_tr_b16 v[58:59], v137 offset:2448\n\t" \
  "v_mfma_f32_32x32x16_bf16 v[16:31], v[108:111], v[84:87], v[16:31]\n\t" \
  "buffer_load_dwordx4 v139, %[rf], %[sk] offen offset:2048 sc1 lds\n\t" \
  "ds_read_b64_tr_b16 v[60:61], v137 offset:2336\n\t" \
  "ds_read_b64_tr_b16 v[62:63], v137 offset:2480\n\t" \
  "v_mfma_f32_32x32x16_bf16 v[0:15], v[112:115], v[88:91], v[0:15]\n\t" \
  "buffer_load_dwordx4 v139, %[rf], %[sk] offen offset:3072 sc1 lds\n\t" \
  "s_add_u32 m0, m0, 4096\n\t" \
  "s_add_u32 %[sk], %[sk], 4096\n\t" \
  "ds_read_b128 v[40:43], v135 offset:0\n\t" \
  "ds_read_b64_tr_b16 v[64:65], v137 offset:4608\n\t" \
  "ds_read_b64_tr_b16 v[66:67], v137 offset:4752\n\t" \
  "v_mfma_f32_32x32x16_bf16 v[16:31], v[116:119], v[88:91], v[16:31]\n\t" \
  "buffer_load_dwordx4 v139, %[rf], %[sk] offen sc1 lds\n\t" \
  "s_add_u32 %[sk], %[sk], 6144\n\t" \
  "ds_read_b64_tr_b16 v[68:69], v137 offset:4640\n\t" \
  "ds_read_b64_tr_b16 v[70:71], v137 offset:4784\n\t" \
  "v_mfma_f32_32x32x16_bf16 v[0:15], v[120:123], v[92:95], v[0:15]\n\t" \
  "ds_read_b128 v[44:47], v136 offset:0\n\t" \
  "ds_read_b64_tr_b16 v[72:73], v137 offset:6912\n\t" \
  "ds_read_b64_tr_b16 v[74:75], v137 offset:7056\n\t" \
  "v_mfma_f32_32x32x16_bf16 v[16:31], v[124:127], v[92:95], v[16:31]\n\t" \
  "ds_read_b64_tr_b16 v[76:77], v137 offset:6944\n\t" \
  "ds_read_b64_tr_b16 v[78:79], v137 offset:7088\n\t" \
  "s_waitcnt lgkmcnt(0)\n\t" \
  "v_mfma_f32_32x32x16_bf16 v[0:15], v[48:51], v[32:35], v[0:15]\n\t" \
  "s_add_u32 m0, %[mk], 61440\n\t" \
  "v_mfma_f32_32x32x16_bf16 v[16:31], v[52:55], v[32:35], v[16:31]\n\t" \
  "buffer_load_dwordx4 v139, %[rf], %[sk] offen offset:0 sc1 lds\n\t" \
  "v_mfma_f32_32x32x16_bf16 v[0:15], v[56:59], v[36:39], v[0:15]\n\t" \
  "buffer_load_dwordx4 v139, %[rf], %[sk] offen offset:1024 sc1 lds\n\t" \
  "v_mfma_f32_32x32x16_bf16 v[16:31], v[60:63], v[36:39], v[16:31]\n\t" \
  "buffer_load_dwordx4 v139, %[rf], %[sk] offen offset:2048 sc1 lds\n\t" \
  "v_mfma_f32_32x32x16_bf16 v[0:15], v[64:67], v[40:43], v[0:15]\n\t" \
  "buffer_load_dwordx4 v139, %[rf], %[sk] offen offset:3072 sc1 lds\n\t" \
  "s_add_u32 m0, m0, 4096\n\t" \
  "s_add_u32 %[sk], %[sk], 4096\n\t" \
  "v_mfma_f32_32x32x16_bf16 v[16:31], v[68:71], v[40:43], v[16:31]\n\t" \
  "buffer_load_dwordx4 v139, %[rf], %[sk] offen sc1 lds\n\t" \
  "s_add_u32 %[sk], %[sk], 6144\n\t" \
  "v_mfma_f32_32x32x16_bf16 v[0:15], v[72:75], v[44:47], v[0:15]\n\t" \
  "v_mfma_f32_32x32x16_bf16 v[16:31], v[76:79], v[44:47], v[16:31]\n\t" \
  "s_nop 15\n\t"
#define QP_CLOB "v32","v33","v34","v35","v36","v37","v38","v39","v40","v41","v42","v43","v44","v45","v46","v47","v48","v49","v50","v51","v52","v53","v54","v55","v56","v57","v58","v59","v60","v61","v62","v63","v64","v65","v66","v67","v68","v69","v70","v71","v72","v73","v74","v75","v76","v77","v78","v79","v80","v81","v82","v83","v84","v85","v86","v87","v88","v89","v90","v91","v92","v93","v94","v95","v96","v97","v98","v99","v100","v101","v102","v103","v104","v105","v106","v107","v108","v109","v110","v111","v112","v113","v114","v115","v116","v117","v118","v119","v120","v121","v122","v123","v124","v125","v126","v127"

constexpr int MRG = 61440;
#define EX(X, i) X[i] = __builtin_amdgcn_exp2f(X[i])
#define EX4(X, i) do { EX(X, i); EX(X, i + 1); EX(X, i + 2); EX(X, i + 3); } while (0)

template <bool F>
DI void wo_unit(char* ws, char* out, unsigned lds0, int tt, int cg, int ex, u64 tag, int wid, int lane, const u32x4 (&w)[36], const u32x4 (&we)[9]) {
  const rsrc_t rws = mk_rsrc(ws, WS_END);
  await<F, 2>(ws, F_O + ((tt >> 1) * 8 + (lane & 7)) * 128, tag, wid);
#pragma unroll
  for (int i = 0; i < 9; ++i) dma16c(rws, lane * 16 + tt * 36864 + wid * 1024, WS_OF + i * 4096, lds0 + (i * 4 + wid) * 1024);
  f32x16 acc = {};
  const unsigned a = lds0 + lane * 16, a2 = lds0 + (lane >> 5) * 512 + (lane & 31) * 8;
  auto frag = [&](int k) { if (k < 32) return lds16(a + k * 1024);
    const u32x2 lo = *(const LAS u32x2*)(size_t)(a2 + k * 1024), hi = *(const LAS u32x2*)(size_t)(a2 + k * 1024 + 256); return (u32x4){lo[0], lo[1], hi[0], hi[1]}; };
  const rsrc_t ro = mk_rsrc(out, SEQ * 2 * ROWB); const unsigned op = (tt * 32 + (lane & 31)) * (2 * ROWB) + (lane >> 5) * 16;
  u32x4 f[36];
  wait_bar<4>();
#pragma unroll
  for (int k = 0; k < 8; ++k) f[k] = frag(k);
  SBAR();
#pragma unroll
  for (int k = 0; k < 12; ++k) { acc = mfma16(w[k], f[k], acc); f[k + 8] = frag(k + 8); asm volatile("" : "+v"(acc)); SBAR(); }
  asm volatile("s_waitcnt vmcnt(0)\n\ts_barrier" ::: "memory");
#pragma unroll
  for (int k = 12; k < 20; ++k) { acc = mfma16(w[k], f[k], acc); f[k + 8] = frag(k + 8); asm volatile("" : "+v"(acc)); SBAR(); }
  if (ex >= 0) {
    f32x16 ae = {}; u32x4 g[9];
#pragma unroll
    for (int k = 0; k < 8; ++k) g[k] = lds16(a + wid * 1024 + k * 4096);
    { const u32x2 lo = *(const LAS u32x2*)(size_t)(a2 + 32768 + wid * 1024), hi = *(const LAS u32x2*)(size_t)(a2 + 32768 + wid * 1024 + 256); g[8] = (u32x4){lo[0], lo[1], hi[0], hi[1]}; }
    SBAR();
#pragma unroll
    for (int k = 20; k < 24; ++k) { acc = mfma16(w[k], f[k], acc); f[k + 8] = frag(k + 8); asm volatile("" : "+v"(acc)); SBAR(); }
#pragma unroll
    for (int k = 0; k < 9; ++k) ae = mfma16(we[k], g[k], ae);
    SBAR();
#pragma unroll
    for (int k = 24; k < 28; ++k) { acc = mfma16(w[k], f[k], acc); f[k + 8] = frag(k + 8); asm volatile("" : "+v"(acc)); SBAR(); }
#pragma unroll
    for (int q = 0; q < 4; ++q) *(LAS f32x4*)(size_t)(a + 36864 + wid * 4096 + q * 1024) = (f32x4){ae[4 * q], ae[4 * q + 1], ae[4 * q + 2], ae[4 * q + 3]};
    SBAR();
#pragma unroll
    for (int k = 28; k < 32; ++k) { acc = mfma16(w[k], f[k], acc); asm volatile("" : "+v"(acc)); SBAR(); }
    lds_bar();
    f32x4 s0[4], s1[4];
#pragma unroll
    for (int q = 0; q < 4; ++q) { s0[q] = *(const LAS f32x4*)(size_t)(a + 36864 + q * 4096 + (wid & 1) * 2048); s1[q] = *(const LAS f32x4*)(size_t)(a + 36864 + q * 4096 + (wid & 1) * 2048 + 1024); }
    SBAR();
#pragma unroll
    for (int k = 32; k < 36; ++k) { acc = mfma16(w[k], f[k], acc); asm volatile("" : "+v"(acc)); SBAR(); }
    const f32x4 t0 = (s0[0] + s0[1]) + (s0[2] + s0[3]), t1 = (s1[0] + s1[1]) + (s1[2] + s1[3]);
    __builtin_amdgcn_raw_buffer_store_b128(pack8(t0[0], t0[1], t0[2], t0[3], t1[0], t1[1], t1[2], t1[3]), ro, wid < 2 ? op + (16 + ex) * 64 + wid * 32 : 0x40000000u, 0, 0);
  } else {
#pragma unroll
    for (int k = 20; k < 36; ++k) { acc = mfma16(w[k], f[k], acc); if (k + 8 < 36) f[k + 8] = frag(k + 8); asm volatile("" : "+v"(acc)); SBAR(); }
  }
  __builtin_amdgcn_raw_buffer_store_b128(pack_pair(acc, 0, 1.f), ro, op + (cg * 4 + wid) * 64, 0, 0);
  __builtin_amdgcn_raw_buffer_store_b128(pack_pair(acc, 1, 1.f), ro, op + (cg * 4 + wid) * 64 + 32, 0, 0);
}
DI void load_wt(char* ws, int cg, int ex, int wid, int lane, u32x4 (&w)[36], u32x4 (&we)[9]) {
  const rsrc_t rws = mk_rsrc(ws, WS_END);
#pragma unroll
  for (int k = 0; k < 36; ++k) {
    w[k] = ld4c(rws, lane * 16 + (k & 3) * 1024, WS_WT + (cg * 4 + wid) * 36864 + (k >> 2) * 4096);
    if ((k & 3) == 3) asm volatile("s_waitcnt vmcnt(%0)" :: "i"(4) : "memory");
  }
#pragma unroll
  for (int k = 0; k < 9; ++k) we[k] = ld4c(rws, lane * 16, ex < 0 ? 0x40000000u : WS_WT + (16 + ex) * 36864 + wid * 1024 + k * 4096);
}

DI float tilemax(const f32x16& s) {
  float a = max3f(s[0], s[1], s[2]), b = max3f(s[3], s[4], s[5]), c = max3f(s[6], s[7], s[8]), d = max3f(s[9], s[10], s[11]);
  a = max3f(a, s[12], s[13]); b = max3f(b, s[14], s[15]);
  return __builtin_fmaxf(max3f(a, b, c), d);
}
DI float xhalf_max(float m) { auto rr = __builtin_amdgcn_permlane32_swap(__float_as_uint(m), __float_as_uint(m), false, false); return __builtin_fmaxf(__uint_as_float(rr[0]), __uint_as_float(rr[1])); }
DI float upper_half(float v) { auto rr = __builtin_amdgcn_permlane32_swap(__float_as_uint(v), __float_as_uint(v), false, false); return __uint_as_float(rr[1]); }
DI unsigned negm_bits(float m, int hf) {
  const float nm = -m;
  const unsigned h = cvtpk(nm, 0.f) & 0xffffu; const float r1 = nm - bflo(h);
  const unsigned md = cvtpk(r1, 0.f) & 0xffffu; const float r2 = r1 - bflo(md);
  const unsigned l = cvtpk(r2, 0.f) & 0xffffu;
  return hf ? l : (h | (md << 16));
}
DI f32x16 attn_exact(rsrc_t rws, u32x4 qa, u32x2 qb, unsigned oK, unsigned oV, unsigned oB, int hf) {
  const f32x16 zero = {}; const int idx = 0xDDDD;
  qb[1] = 0u;
  float mx = -__builtin_inff();
  for (int t = 0; t < 64; ++t) {
    const u32x4 ka = ld4c(rws, oK, WS_KV + t * KVT); const u32x2 kb = {ld1c(rws, oB, WS_KV + t * KVT), 0x3f803f80u};
    f32x16 s = mfma16(ka, qa, zero); s = mfma8(kb, qb, s);
    mx = __builtin_fmaxf(mx, tilemax(s));
  }
  qb[1] = negm_bits(xhalf_max(mx), hf);
  f32x16 o = {};
  for (int t = 0; t < 64; ++t) {
    const u32x4 ka = ld4c(rws, oK, WS_KV + t * KVT); const u32x2 kb = {ld1c(rws, oB, WS_KV + t * KVT), 0x3f803f80u};
    const u32x4 v0 = ld4c(rws, oV, WS_KV + t * KVT), v1 = ld4c(rws, oV + 640, WS_KV + t * KVT);
    f32x16 s = mfma16(ka, qa, zero); s = mfma8(kb, qb, s);
    EX4(s, 0); EX4(s, 4); EX4(s, 8); EX4(s, 12);
    o = smf(v0, lo8(s), o, idx); o = smf(v1, hi8(s), o, idx);
  }
  return o;
}

struct QSrc { unsigned vx0, vx1, vw0, vw1, vw2; };
DI unsigned mul24(unsigned a, unsigned b) { return __umul24(a, b); }
DI QSrc q_src(int wid, unsigned lane) {
  QSrc q; unsigned vw[3];
  const unsigned xr = mul24(wid * 16 + (lane >> 3), ROWB);
  q.vx0 = xr + (((lane & 7) ^ (lane >> 4)) << 4); q.vx1 = xr + 8 * ROWB - 1024 + (((lane & 7) ^ (4 + (lane >> 4))) << 4);
#pragma unroll
  for (int i = 0; i < 3; ++i) {
    const unsigned n = (wid * 2 + i) * 64 + lane, pos = mul24(n, 7282) >> 16, row = (pos & ~15u) + 4 * (pos & 3) + ((pos >> 2) & 3);
    vw[i] = mul24(row, ROWB) + (n - mul24(pos, 9)) * 16 - i * 1024;
  }
  q.vw0 = vw[0]; q.vw1 = vw[1]; q.vw2 = vw[2];
  return q;
}
DI void q_issue(int tg, int hg, const char* xa, const char* wq, unsigned lds0, int wid, int lane) {
  const QSrc q = q_src(wid, lane);
  unsigned sxo = tg * 64 * ROWB, swo = hg * 144;
  asm volatile("s_nop 4\n\t" QP0_ASM : [sx] "+&s"(sxo), [sw] "+&s"(swo)
    : "{v128}"(q.vx0), "{v129}"(q.vx1), "{v130}"(q.vw0), "{v131}"(q.vw1), "{v132}"(q.vw2),
      [rx] "s"(mk_rsrc(xa, SEQ * ROWB)), [rw] "s"(mk_rsrc(wq, DM * ROWB)), [mx] "s"(lds0 + 12288 + wid * 2048), [ex] "s"((wid + 1) >> 2) : "scc", "memory");
}
template <bool F>
DI u64 attn_phase(int tg, int hg, const char* xa, const char* wq, char* ws, unsigned lds0, int wid, int lane, unsigned pfo, u64& pf) {
  const int r = lane & 31, hf = lane >> 5, rg = wid >> 1, ks = wid & 1;
  const f32x16 zero = {};
  u32x4 qA[4]; u32x2 qB[4];
  u64 tag; u32x4 kvf;
  const rsrc_t rws = mk_rsrc(ws, WS_END);
  const unsigned kv0 = WS_KV + ks * 32 * KVT;
  const unsigned ring = lds0 + ks * 10240, vd = rg * 5120 + lane * 16;
  {
    const rsrc_t rx = mk_rsrc(xa, SEQ * ROWB), rw = mk_rsrc(wq, DM * ROWB);
    const QSrc q = q_src(wid, lane);
    const unsigned ax = lds0 + 12288 + (rg * 32 + r) * 128, sx = (r >> 1) & 7;
    const unsigned aw = lds0 + 12288 + 8192 + (2 * hf + (lane & 12)) * 144 + 72 * ks + 32 * ((lane >> 4) & 1) + 8 * (lane & 3);
    unsigned sxo = tg * 64 * ROWB + 512, swo = hg * 144 + 4 * 73728, sk = kv0; int cnt; u64 t2;
    f32x16 acc0, acc1;
    asm volatile("s_nop 4\n\t" QP_ASM
      : "=&{v[0:15]}"(acc0), "=&{v[16:31]}"(acc1), "=&{v[140:141]}"(tag), "=&{v[142:145]}"(kvf), [sx] "+&s"(sxo), [sw] "+&s"(swo), [sk] "+&s"(sk), [cnt] "=&s"(cnt), [t2] "=&s"(t2)
      : "{v128}"(q.vx0), "{v129}"(q.vx1), "{v130}"(q.vw0), "{v131}"(q.vw1), "{v132}"(q.vw2),
        "{v133}"(ax + ((hf ^ sx) << 4)), "{v134}"(ax + (((2 + hf) ^ sx) << 4)), "{v135}"(ax + (((4 + hf) ^ sx) << 4)), "{v136}"(ax + (((6 + hf) ^ sx) << 4)), "{v137}"(aw), "{v138}"(wid ? 0x40000000u : F_R + ((tg * 8 + hg) % 32) * 1024 + lane * 16), "{v139}"(vd), [mk] "s"(ring + rg * 5120), [fm] "s"(F ? (u64)(long long)((wid - 1) >> 31) : (u64)0),
        [rf] "s"(rws), [rx] "s"(rx), [rw] "s"(rw), [mx] "s"(lds0 + 12288 + wid * 2048), [ex] "s"((wid + 1) >> 2)
      : "scc", "vcc", "memory", QP_CLOB);
    qA[0] = pack_pair(acc0, 0, QSCALE);
    qB[0][0] = hf ? 0u : cvtpk(acc0[8] * QSCALE, acc0[9] * QSCALE);
    unsigned p[6];
#pragma unroll
    for (int i = 0; i < 6; ++i) p[i] = cvtpk(acc1[2 * i] * QSCALE, acc1[2 * i + 1] * QSCALE);
    auto e0 = __builtin_amdgcn_permlane32_swap(p[0], p[3], false, false); auto e1 = __builtin_amdgcn_permlane32_swap(p[1], p[4], false, false);
    qA[1] = (u32x4){hf ? e0[0] : e1[0], hf ? p[2] : e0[1], hf ? e0[1] : e1[1], hf ? e1[0] : p[2]};
    qB[1][0] = hf ? 0u : p[5];
  }
  {
    const unsigned xo = lds0 + lane * 16;
    *(LAS u32x4*)(size_t)(xo + wid * 3072) = qA[0]; *(LAS u32x4*)(size_t)(xo + wid * 3072 + 1024) = qA[1];
    *(LAS u32x2*)(size_t)(xo + wid * 3072 + 2048) = (u32x2){qB[0][0], qB[1][0]};
    tag = uni64(tag);
    wait_bar<5>();
    qA[2] = lds16(xo + (wid ^ 1) * 3072); qA[3] = lds16(xo + (wid ^ 1) * 3072 + 1024);
    const u32x2 t = *(const LAS u32x2*)(size_t)(xo + (wid ^ 1) * 3072 + 2048); qB[2][0] = t[0]; qB[3][0] = t[1];
  }
  const unsigned oK = lane * 16, oV = 1024 + (hf * 20 + (r < 18 ? r : (r == 20 ? 18 : 19))) * 16, oB = 2304 + r * 4;
  {
    const u32x4 sa = lds16(lds0 + 40960 + oK); f32x16 sm[4];
#pragma unroll
    for (int i = 0; i < 4; ++i) sm[i] = mfma16(sa, qA[i], zero);
#pragma unroll
    for (int i = 0; i < 4; ++i) qB[i][1] = cvtpk(-(xhalf_max(tilemax(sm[i])) + MSHIFT), 0.f) & (hf - 1u);
  }
  u32x4 ka0 = lds16(ring + 40960 + oK), va0 = lds16(ring + 40960 + oV), vb0 = lds16(ring + 40960 + oV + 640), ka1, va1, vb1 = {0u, 0u, 0u, 0u};
  u32x2 kb0 = {lds4(ring + 40960 + oB), 0x3f803f80u}, kb1 = {0u, 0x3f803f80u};
  f32x16 o0 = {}, o1 = {}, o2 = {}, o3 = {}, sX, sY; f32x8 hP = {};
  sX = mfma16(ka0, qA[0], zero); sX = mfma8(kb0, qB[0], sX);
  lds_bar();
  PRIO(1);
  {
    unsigned so = kv0 + 20480, sl = 40960, dl, aK = ring + 40960 + oK, aV = ring + 40960 + oV, aB = ring + 40960 + oB; int cnt; const int idx = 0xDDDD;
    asm volatile("s_nop 15\n\t" ATTN_LOOP_ASM
      : "+{v[0:15]}"(o0), "+{v[16:31]}"(o1), "+{v[32:47]}"(o2), "+{v[48:63]}"(o3), "+{v[64:79]}"(sX), "=&{v[80:95]}"(sY), "+{v[96:103]}"(hP),
        "+{v[128:131]}"(ka0), "=&{v[132:135]}"(ka1), "+{v[136:137]}"(kb0), "+{v[138:139]}"(kb1),
        "+{v[140:143]}"(va0), "+{v[144:147]}"(vb0), "=&{v[148:151]}"(va1), "+{v[152:155]}"(vb1),
        "+{v157}"(aK), "+{v158}"(aV), "+{v159}"(aB),
        [so] "+&s"(so), [sl] "+&s"(sl), [dl] "=&s"(dl), [cnt] "=&s"(cnt)
      : "{v[104:107]}"(qA[0]), "{v[108:111]}"(qA[1]), "{v[112:115]}"(qA[2]), "{v[116:119]}"(qA[3]),
        "{v[120:121]}"(qB[0]), "{v[122:123]}"(qB[1]), "{v[124:125]}"(qB[2]), "{v[126:127]}"(qB[3]),
        "{v156}"(idx), "{v160}"(vd), "{v161}"(vd + 4096), [rs] "s"(rws), [mb] "s"(ring + rg * 5120 + 40960), [mc] "s"(ring + rg * 5120 - 40960), [cn] "s"(-61440)
      : "scc", "vcc", "memory");
  }
  PRIO(3);
  if (F) pf = ldflag(ws, pfo);
  {
    const unsigned xo = lds0 + MRG + wid * 5120 + lane * 16, xo8 = lds0 + MRG + wid * 5120 + 2048 + lane * 8;
    *(LAS f32x4*)(size_t)(xo) = (f32x4){o2[0], o2[1], o2[2], o2[3]}; *(LAS f32x4*)(size_t)(xo + 1024) = (f32x4){o2[4], o2[5], o2[6], o2[7]};
    *(LAS f32x2*)(size_t)(xo8) = (f32x2){o2[8], o2[9]};
    *(LAS f32x4*)(size_t)(xo + 2560) = (f32x4){o3[0], o3[1], o3[2], o3[3]}; *(LAS f32x4*)(size_t)(xo + 2560 + 1024) = (f32x4){o3[4], o3[5], o3[6], o3[7]};
    *(LAS f32x2*)(size_t)(xo8 + 2560) = (f32x2){o3[8], o3[9]};
    lds_bar();
    const unsigned yo = lds0 + MRG + (wid ^ 1) * 5120 + lane * 16, yo8 = lds0 + MRG + (wid ^ 1) * 5120 + 2048 + lane * 8;
#define GET(OO, d) do { const f32x4 a = *(const LAS f32x4*)(size_t)(yo + (d)), b = *(const LAS f32x4*)(size_t)(yo + (d) + 1024); const f32x2 c = *(const LAS f32x2*)(size_t)(yo8 + (d)); \
      OO[0] += a[0]; OO[1] += a[1]; OO[2] += a[2]; OO[3] += a[3]; OO[4] += b[0]; OO[5] += b[1]; OO[6] += b[2]; OO[7] += b[3]; OO[8] += c[0]; OO[9] += c[1]; } while (0)
    GET(o0, 0); GET(o1, 2560);
  }
  auto unsafe = [&](const f32x16& o) {
    const float l = upper_half(o[8]); float chk = 0.f;
#pragma unroll
    for (int g = 0; g < 8; ++g) chk += __builtin_fabsf(o[g]);
    return __any(!(l < 1.0e36f) || !(l > 1.0e-28f) || !(chk > 1.0e-30f) || !(chk + __builtin_fabsf(o[8]) + __builtin_fabsf(o[9]) < 1.0e38f)) != 0;
  };
  const unsigned ob = WS_OF + (tg * 2 + rg) * 36864, oh = ob + (hg * 4 + ks * 2) * 1024, ol = ob + 32768 + hg * 512 + ks * 256;
  const float r0 = __builtin_amdgcn_rcpf(upper_half(o0[8])), r1 = __builtin_amdgcn_rcpf(upper_half(o1[8]));
  st4c(pack_pair(o0, 0, r0), rws, lane * 16, oh);
  st4c(pack_pair(o1, 0, r1), rws, lane * 16, oh + 1024);
  if (hf == 0) st2c((u32x2){cvtpk(o0[8] * r0, o0[9] * r0), cvtpk(o1[8] * r1, o1[9] * r1)}, rws, r * 8, ol);
  const int bad = (unsafe(o0) ? 1 : 0) | (unsafe(o1) ? 2 : 0);
  if (__builtin_expect(bad != 0, 0)) {
#pragma nounroll
    for (int i = 0; i < 2; ++i)
      if ((bad >> i) & 1) {
        const f32x16 o = attn_exact(rws, i ? qA[1] : qA[0], i ? qB[1] : qB[0], oK, oV, oB, hf);
        const float rl = 1.0f / upper_half(o[8]);
        st4c(pack_pair(o, 0, rl), rws, lane * 16, oh + i * 1024);
        if (hf == 0) st1c(cvtpk(o[8] * rl, o[9] * rl), rws, r * 8, ol + i * 4);
      }
  }
  publish<F>(ws, F_O + (tg * 8 + hg) * 128, tag);
  return tag;
}

template <bool F>
DI u64 hid_phase(int x, int j, const char* xb, const char* dre, const char* dim, char* ws, unsigned lds0, int wid, int lane) {
  const int sp = x >> 1, sl = (x & 1) * 4 + j / 6, st = (sp & 1) * 8 + sl, et = j % 6, im = sp >> 1;
  const rsrc_t rc = mk_rsrc(xb, SEQ * ROWB), rb = mk_rsrc(im ? dim : dre, DM * ROWB);
  unsigned vx[4], vd[3];
#pragma unroll
  for (int q = 0; q < 4; ++q) {
    const int i = (q & 1) * 8 + (lane >> 3);
    vx[q] = (wid * 32 + 8 * (i >> 2) + 4 * (q >> 1) + (i & 3)) * ROWB + (((lane & 7) ^ ((4 * q + (lane >> 4)) & 7)) << 4) - q * 1024;
    if (q < 3) vd[q] = (wid * 24 + q * 8 + (lane >> 3)) * ROWB + (((lane & 7) ^ ((12 * wid + 4 * q + (lane >> 4)) & 7)) << 4) - q * 1024;
  }
  const unsigned rd = (lane & 15) * 128 + (((lane >> 4) ^ ((lane & 15) >> 1)) << 4);
  unsigned sc = st * 128 * ROWB, sb = et * 96 * ROWB; int cnt;
  f32x16 a0 = {}, a1 = {}, a2 = {};
  PRIO(2);
  asm volatile("s_nop 4\n\t" B1_LOOP_ASM
    : "+{v[0:15]}"(a0), "+{v[16:31]}"(a1), "+{v[32:47]}"(a2), [sc] "+&s"(sc), [sb] "+&s"(sb), [cnt] "=&s"(cnt)
    : "{v176}"(vx[0]), "{v177}"(vx[1]), "{v178}"(vx[2]), "{v179}"(vx[3]), "{v180}"(vd[0]), "{v181}"(vd[1]), "{v182}"(vd[2]),
      "{v183}"(lds0 + wid * 4096 + rd), "{v184}"(lds0 + wid * 4096 + (rd ^ 64)), "{v185}"(lds0 + 16384 + rd), "{v186}"(lds0 + 16384 + (rd ^ 64)),
      [rc] "s"(rc), [rb] "s"(rb), [mc] "s"(lds0 + wid * 4096), [mb] "s"(lds0 + 16384 + wid * 3072)
    : "scc", "memory", B1_CLOB);
  PRIO(2);
  u64 tag = 0;
  if (F) tag = ldflag(ws, F_EPOCH);
  const rsrc_t rws = mk_rsrc(ws, WS_END);
  const float sg = im ? -1.f : 1.f;
  const unsigned bo = WS_BT + ((im * 64 + st * 4 + wid) * 36 + et * 6) * 1024;
#define BST(A, o, e) st4c((u32x4){cvtpk(A[o] * sg, A[o + 1] * sg), cvtpk(A[o + 2] * sg, A[o + 3] * sg), cvtpk(A[o + 4] * sg, A[o + 5] * sg), cvtpk(A[o + 6] * sg, A[o + 7] * sg)}, rws, lane * 16, bo + (e) * 1024)
  BST(a0, 0, 0); BST(a0, 8, 1); BST(a1, 0, 2); BST(a1, 8, 3); BST(a2, 0, 4); BST(a2, 8, 5);
  tag = uni64(tag) + 1;
  publish<F>(ws, F_B1 + (sp * 48 + sl * 6 + et) * 128, tag);
  return tag;
}

DI void kv_issue(int u, const char* xa, const char* wkv, unsigned lds0, int wid, unsigned lane) {
  const rsrc_t rx = mk_rsrc(xa, SEQ * ROWB), rw = mk_rsrc(wkv, DM * 36);
  const unsigned lw = lds0 + wid * 16384;
#pragma unroll
  for (int i = 0; i < 9; ++i) { const unsigned n = i * 64 + lane, row = mul24(n, 3641) >> 16; dma16(rx, mul24(row, ROWB - 288) + n * 16, u * 32 * ROWB + wid * 288, lw + i * 1024); }
#pragma unroll
  for (int i = 0; i < 7; ++i) { const unsigned n = i * 64 + lane, row = mul24(n, 21846) >> 16; dma16(rw, n * 16 - mul24(row, 12), wid * 5184, lw + 9216 + i * 1024); }
}
template <bool F>
DI u64 kv_phase(int u, int mat, char* ws, unsigned lds0, int wid, int lane) {
  const int r = lane & 31, hf = lane >> 5;
  const unsigned lw = lds0 + wid * 16384;
  u64 tag = 0;
  if (F) tag = ldflag(ws, F_EPOCH);
  const unsigned ax = lw + r * 288 + hf * 16, aw = lw + 9216 + (8 * hf + ((lane & 15) >> 2)) * 48 + 32 * ((lane >> 4) & 1) + 8 * (lane & 3);
  asm volatile("s_waitcnt vmcnt(0)" ::: "memory");
  u32x4 fx[9], fw[9];
#pragma unroll
  for (int k = 0; k < 9; ++k) { fx[k] = lds16(ax + k * 32); const u32x2 t0 = ldstr(aw + k * 768), t1 = ldstr(aw + k * 768 + 192); fw[k] = (u32x4){t0[0], t0[1], t1[0], t1[1]}; }
  SBAR();
  f32x16 acc = {};
  if (mat) {
#pragma unroll
    for (int k = 0; k < 9; ++k) acc = mfma16(fx[k], fw[k], acc);
  } else {
#pragma unroll
    for (int k = 0; k < 9; ++k) acc = mfma16(fw[k], fx[k], acc);
  }
#pragma unroll
  for (int g = 0; g < 4; ++g) *(LAS f32x4*)(size_t)(lw + g * 1024 + lane * 16) = (f32x4){acc[4 * g], acc[4 * g + 1], acc[4 * g + 2], acc[4 * g + 3]};
  lds_bar();
  const rsrc_t rws = mk_rsrc(ws, WS_END);
  if (wid < 2) {
    f32x4 s0 = {}, s1 = {};
#pragma unroll
    for (int w = 0; w < 4; ++w) { s0 += *(const LAS f32x4*)(size_t)(lds0 + w * 16384 + wid * 2048 + lane * 16); s1 += *(const LAS f32x4*)(size_t)(lds0 + w * 16384 + wid * 2048 + 1024 + lane * 16); }
    u32x4 p = pack8(s0[0], s0[1], s0[2], s0[3], s1[0], s1[1], s1[2], s1[3]);
    if (mat) {
      const unsigned cst = r == 18 ? 0x3f803f80u : 0u;
      if (r >= 18) p = (u32x4){cst, cst, cst, cst};
      if (r < 20) st4c(p, rws, 1024 + wid * 640 + (hf * 20 + r) * 16, WS_KV + u * KVT);
    } else if (wid == 0) st4c(p, rws, lane * 16, WS_KV + u * KVT);
    else if (hf == 0) st1c(p[0], rws, 2304 + r * 4, WS_KV + u * KVT);
  }
  tag = uni64(tag) + 1;
  asm volatile("s_waitcnt vmcnt(0) lgkmcnt(0)\n\ts_barrier" ::: "memory");
  if (F && threadIdx.x <= 32) {
    const unsigned fo = threadIdx.x < 32 ? F_R + threadIdx.x * 1024 + u * 16 + mat * 8 : (mat ? F_V : F_KV) + u * 128;
    (void)__hip_atomic_exchange((GAS u64*)(ws + fo), tag, __ATOMIC_RELAXED, __HIP_MEMORY_SCOPE_AGENT);
  }
  return tag;
}
template <bool F>
DI void wt_phase(int u, const char* wo, char* ws, u64 tag, int wid, int lane) {
  const int r = lane & 31, hf = lane >> 5;
  const rsrc_t rws = mk_rsrc(ws, WS_END);
  for (int f = u * 4 + wid; f < 648; f += 256) {
    const int nt = f / 36, kq = f - nt * 36;
    u16x8 wf;
#pragma unroll
    for (int e = 0; e < 8; ++e) {
      const int sl = hf * 8 + e, row = kq < 32 ? kq * 18 + sl : ((kq - 32) * 8 + (sl >> 1)) * 18 + 16 + (sl & 1);
      wf[e] = *(const unsigned short*)(wo + (size_t)row * ROWB + (nt * 32 + r) * 2);
    }
    st4c(__builtin_bit_cast(u32x4, wf), rws, lane * 16, WS_WT + f * 1024);
  }
  publish<F>(ws, F_WT + u * 128, tag);
}

template <bool F>
DI void seq_phase(int x, int j, const char* cre, const char* cim, char* ws, unsigned lds0, u64 tag, int wid, int lane) {
  asm volatile("" : "+s"(x), "+s"(j), "+s"(wid));
  const int sp = x >> 1, mt = (x & 1) * 8 + (j >> 2), nt = j & 3;
  const rsrc_t rc = mk_rsrc(sp < 2 ? cre : cim, SEQ * SEQ * 2), rb = mk_rsrc(ws + WS_BT, 128 * 36 * 1024);
  if (F && wid == 1) poll<16>(ws, F_WT + lane * 128, tag);
  await<F, 8>(ws, F_B1 + (sp * 48 + ((lane >> 1) & 7) * 6 + (nt * 3 >> 1) + (lane & 1)) * 128, tag, wid);
  unsigned vc[4];
#pragma unroll
  for (int q = 0; q < 4; ++q) vc[q] = (wid * 32 + q * 8 + (lane >> 3)) * 4096 + (((lane & 7) ^ ((4 * q + (lane >> 4)) & 7)) << 4) - q * 1024;
  const unsigned rd = (lane & 15) * 128 + (((lane >> 4) ^ ((lane & 15) >> 1)) << 4);
  const unsigned sc0 = mt * 128 * 4096 + (sp & 1) * 2048, sb0 = (sp * 32 * 36 + nt * 9) * 1024;
  unsigned kc = (mt & 7) * 256, kb = kc, sc = sc0 + kc, sb = sb0 + kb * 576; int cnt;
  f32x16 a0 = {}, a1 = {}, a2 = {}, a3 = {}; f32x8 a4 = {};
  PRIO(2);
  asm volatile("s_nop 4\n\t" B2_LOOP_ASM
    : "+{v[0:15]}"(a0), "+{v[16:31]}"(a1), "+{v[32:47]}"(a2), "+{v[48:63]}"(a3), "+{v[64:71]}"(a4), [sc] "+&s"(sc), [sb] "+&s"(sb), [kc] "+&s"(kc), [kb] "+&s"(kb), [cnt] "=&s"(cnt)
    : "{v176}"(vc[0]), "{v177}"(vc[1]), "{v178}"(vc[2]), "{v179}"(vc[3]), "{v180}"(wid * 2048 + lane * 16),
      "{v181}"(lds0 + wid * 4096 + rd), "{v182}"(lds0 + wid * 4096 + (rd ^ 64)), "{v183}"(lds0 + 32768 + lane * 16),
      [rc] "s"(rc), [rb] "s"(rb), [mc] "s"(lds0 + wid * 4096), [mb] "s"(lds0 + 32768 + wid * 2048), [ex] "s"((wid + 1) >> 2), [sc0] "s"(sc0), [sb0] "s"(sb0)
    : "scc", "memory", B2_CLOB);
  PRIO(2);
  const rsrc_t rws = mk_rsrc(ws, WS_END);
  const unsigned po = WS_P + sp * SEQ * ROWB + ((mt * 4 + nt) * 4 + wid) * 9216;
#define PST(A, o, e) st4c((u32x4){cvtpk(A[o], A[o + 1]), cvtpk(A[o + 2], A[o + 3]), cvtpk(A[o + 4], A[o + 5]), cvtpk(A[o + 6], A[o + 7])}, rws, lane * 16, po + (e) * 1024)
  PST(a0, 0, 0); PST(a0, 8, 1); PST(a1, 0, 2); PST(a1, 8, 3); PST(a2, 0, 4); PST(a2, 8, 5); PST(a3, 0, 6); PST(a3, 8, 7); PST(a4, 0, 8);
  publish<F>(ws, F_P + ((mt * 4 + nt) * 4 + sp) * 128, tag);
}
template <bool F>
DI void combine_phase(int x, int j, char* ws, char* out, unsigned lds0, u64 tag, int wid, int lane, unsigned pfo, u64 pf) {
  const int sp = x >> 1, mt = (x & 1) * 8 + (j >> 2), nt = j & 3;
  if (F && !__all(pf == tag)) poll<2>(ws, pfo, tag);
  __builtin_amdgcn_fence(__ATOMIC_ACQUIRE, "wavefront");
  const rsrc_t rws = mk_rsrc(ws, WS_END), ro = mk_rsrc(out, SEQ * 2 * ROWB);
  const unsigned po = WS_P + ((mt * 4 + nt) * 4 + sp) * 9216 + wid * 1024;
  u32x4 v[3][4];
#pragma unroll
  for (int i = 0; i < 3; ++i)
#pragma unroll
    for (int s = 0; s < 4; ++s) v[i][s] = ld4c(rws, lane * 16, i == 2 && wid ? 0x40000000u : po + s * SEQ * ROWB + i * 4096);
#pragma unroll
  for (int i = 0; i < 3; ++i)
    if (i < 2 || wid == 0) {
      float s[8];
#pragma unroll
      for (int e = 0; e < 4; ++e) { s[2 * e] = (bflo(v[i][0][e]) + bflo(v[i][1][e])) + (bflo(v[i][2][e]) + bflo(v[i][3][e])); s[2 * e + 1] = (bfhi(v[i][0][e]) + bfhi(v[i][1][e])) + (bfhi(v[i][2][e]) + bfhi(v[i][3][e])); }
      const unsigned la = lds0 + (lane & 15) * 304 + (wid + i * 4) * 32 + (lane >> 4) * 8;
      *(LAS u32x2*)(size_t)la = (u32x2){cvtpk(s[0], s[1]), cvtpk(s[2], s[3])};
      *(LAS u32x2*)(size_t)(la + 16 * 304) = (u32x2){cvtpk(s[4], s[5]), cvtpk(s[6], s[7])};
    }
  lds_bar();
#pragma unroll
  for (int i = 0; i < 3; ++i) {
    const int n = i * 256 + threadIdx.x, row = n / 18, c = n - row * 18;
    if (n < 576) __builtin_amdgcn_raw_buffer_store_b128(lds16(lds0 + row * 304 + c * 16), ro, (mt * 128 + sp * 32 + row) * (2 * ROWB) + ROWB + nt * 288 + c * 16, 0, 0);
  }
}

template <int MODE>
__global__ NOPK __launch_bounds__(256, 2) void fused_kernel(const char* __restrict__ xa, const char* __restrict__ wq, const char* __restrict__ wk, const char* __restrict__ wv,
                                                       const char* __restrict__ wo, const char* __restrict__ xb, const char* __restrict__ cre, const char* __restrict__ cim,
                                                       const char* __restrict__ dre, const char* __restrict__ dim, char* ws, char* out) {
  __shared__ __attribute__((aligned(16))) char lds[LDS_BYTES];
  constexpr bool F = MODE == 3;
  const unsigned lds0 = (unsigned)(size_t)(LAS char*)lds;
  const int lane = threadIdx.x & 63, wid = __builtin_amdgcn_readfirstlane(threadIdx.x >> 6);
  int b = (blockIdx.x + 128) & 511;
  if (blockIdx.x < 8) {
    u64 pc; asm volatile("s_getpc_b64 %0" : "=s"(pc));
    asm volatile("s_mov_b32 m0, %0\n\ts_nop 0\n\tbuffer_load_dword %1, %2, 0 offen lds" :: "s"(lds0 + 81664), "v"(threadIdx.x * 128), "s"(mk_rsrc((const void*)(pc & ~(u64)255), CODE_BYTES)) : "memory");
  }
  PRIO(3);
  if (b >= 256) { if (F || MODE == 1) q_issue(4 * (b & 7) + ((b >> 6) & 3), (b >> 3) & 7, xa, wq, lds0, wid, lane); }
  else if (b >= 128 && (F || MODE == 0)) kv_issue((b & 7) * 8 + ((b >> 3) & 7), xa, b < 192 ? wv : wk, lds0, wid, lane);
  asm volatile("" : "+s"(b));
  const int x = b & 7, j = (b >> 3) & 31;
  u64 tag = 0;
  if (b >= 256) {
    const unsigned pfo = F_P + (((x & 1) * 32 + j) * 4 + (lane & 3)) * 128; u64 pf = 0;
    if (F || MODE == 1) tag = attn_phase<F>(4 * x + (j >> 3), j & 7, xa, wq, ws, lds0, wid, lane, pfo, pf);
    if (F || MODE == 2) combine_phase<F>(x, j, ws, out, lds0, tag, wid, lane, pfo, pf);
    if (F && b == 448) {
      poll<8>(ws, F_KV + (wid * 64 + lane) * 128, tag); poll<8>(ws, F_O + (wid * 64 + lane) * 128, tag);
      lds_bar();
      if (threadIdx.x == 0) stflag(ws, F_EPOCH, tag);
    }
  } else {
    if (F || MODE == 0) {
      if (j >= 16) tag = kv_phase<F>(x * 8 + (j & 7), j < 24, ws, lds0, wid, lane);
      if (j < 24) tag = hid_phase<F>(x, j, xb, dre, dim, ws, lds0, wid, lane);
      else wt_phase<F>(x * 8 + (j & 7), wo, ws, tag, wid, lane);
    }
    if (F || MODE == 1) seq_phase<F>(x, j, cre, cim, ws, lds0, tag, wid, lane);
    if (F || MODE == 2) {
      const int cg = j & 3, ex = (cg & 1) ? -1 : (cg >> 1);
      u32x4 w[36], we[9];
      load_wt(ws, cg, ex, wid, lane, w, we);
      wo_unit<F>(ws, out, lds0, x * 8 + (j >> 2), cg, ex, tag, wid, lane, w, we);
    }
  }
}

extern "C" void kernel_launch(void* const* d_in, const int* in_sizes, int n_in, void* d_out, int out_size, void* d_ws, size_t ws_size, hipStream_t stream) {
  const char* const* in = (const char* const*)d_in;
#define ARGS in[0], in[1], in[2], in[3], in[4], in[5], in[6], in[7], in[8], in[9], (char*)d_ws, (char*)d_out
  fused_kernel<3><<<512, 256, 0, stream>>>(ARGS);
}
```
